```python
import torch

def _softmax(x, axis=-1):
    return torch.softmax(x, dim=axis)

def _gelu(x):
    return torch.nn.functional.gelu(x, approximate='tanh')

def _silu(x):
    return torch.nn.functional.silu(x)

def _sigmoid(x):
    return torch.sigmoid(x)

def _layernorm(x, axis=-1, eps=1e-05):
    m = x.mean(dim=axis, keepdim=True)
    v = x.var(dim=axis, keepdim=True, unbiased=False)
    return (x - m) / torch.sqrt(v + eps)

def _scatter_add(tgt, idx, src, axis=0):
    return tgt.clone().index_add_(axis, idx, src)

def _segment_sum(src, seg, n):
    out = torch.zeros((n,) + src.shape[1:], dtype=src.dtype)
    return out.index_add_(0, seg, src)

def _pad(x, pw):
    flat = tuple((v for pair in reversed(pw) for v in pair))
    return torch.nn.functional.pad(x, flat)
kernel_name = 'adaLN_cond__ssm_conv_h3__geglu_ffn__prompt'

def setup_inputs():

    _w = lambda a: (lambda t: t.to(torch.bfloat16) if t.is_floating_point() else t)(torch.as_tensor(a))

    def _A_setup():
        B, S, D = (1, 2048, 448)
        ks = 4
        return {'q': _w((lambda *s: torch.randn(*s, dtype=torch.bfloat16) * 0.05)(B, S, D)), 'k': _w((lambda *s: torch.randn(*s, dtype=torch.bfloat16) * 0.8)(B, S, D)), 'v': _w((lambda *s: torch.randn(*s, dtype=torch.bfloat16) * 0.8)(B, S, D)), 'shift_k': _w((lambda *s: torch.randn(*s, dtype=torch.bfloat16) * 0.8)(D, ks) * 0.5), 'ssm_k': _w((lambda *s: torch.randn(*s, dtype=torch.bfloat16) * 0.8)(D, S) * (1.0 / S ** 0.5)), 'ssm_D': _w((lambda *s: torch.randn(*s, dtype=torch.bfloat16) * 0.8)(D))}
    _A_in = _A_setup()

    def _B_setup():
        B, S, D, F = (1, 2048, 448, 1792)
        return {'x': _w((lambda *s: torch.randn(*s, dtype=torch.bfloat16) * 0.05)(B, S, D)), 'w_gate': _w((lambda *s: torch.randn(*s, dtype=torch.bfloat16) * 0.05)(D, F)), 'w_up': _w((lambda *s: torch.randn(*s, dtype=torch.bfloat16) * 0.05)(D, F)), 'w_down': _w((lambda *s: torch.randn(*s, dtype=torch.bfloat16) * 0.05)(F, D))}
    _B_in = _B_setup()
    return {'A_q': _A_in['q'], 'A_k': _A_in['k'], 'A_v': _A_in['v'], 'A_shift_k': _A_in['shift_k'], 'A_ssm_k': _A_in['ssm_k'], 'A_ssm_D': _A_in['ssm_D'], 'B_x': _B_in['x'], 'B_w_gate': _B_in['w_gate'], 'B_w_up': _B_in['w_up'], 'B_w_down': _B_in['w_down'], 'c': _w((lambda *s: torch.randn(*s, dtype=torch.bfloat16) * 0.1)(1, 448) * 8.0), 'w_mod': _w((lambda *s: torch.randn(*s, dtype=torch.bfloat16) * 0.1)(448, 6 * 448) * 448 ** (-0.5) * 8.0)}

def reference(A_q, A_k, A_v, A_shift_k, A_ssm_k, A_ssm_D, B_x, B_w_gate, B_w_up, B_w_down, c, w_mod):

    def _A_ref(q, k, v, shift_k, ssm_k, ssm_D):
        B, S, D = q.shape
        ks = shift_k.shape[-1]
        k_pad = _pad(k, ((0, 0), (ks - 1, 0), (0, 0)))
        k_shift = torch.zeros_like(k)
        for t in range(ks):
            k_shift = k_shift + k_pad[:, t:t + S, :] * shift_k[:, ks - 1 - t][None, None, :]
        kv = k_shift * v
        L = 2 * S
        kv_pad = _pad(kv, ((0, 0), (0, S), (0, 0)))
        kf_pad = _pad(ssm_k, ((0, 0), (0, S)))
        kv_f = (lambda x, axes=(-2, -1): torch.fft.rfft2(x.float(), dim=axes))(kv_pad[:, None, :, :], axes=(1, 2))
        kf_f = (lambda x, axes=(-2, -1): torch.fft.rfft2(x.float(), dim=axes))(kf_pad[:, None, :], axes=(1, 2))
        kf_f = (lambda x, *d: x.permute(*d))(kf_f, 1, 2, 0)
        y_f = kv_f * kf_f[None, :, :, :]
        y = (lambda x, s, axes=(-2, -1): torch.fft.irfft2(x, s=s, dim=axes).to(torch.bfloat16))(y_f, (1, L), axes=(1, 2))[:, 0, :S, :]
        y = y + kv * ssm_D[None, None, :]
        return q * y

    def _B_ref(x, w_gate, w_up, w_down):
        return _gelu(x @ w_gate) * (x @ w_up) @ w_down
    x = A_q
    mod = (_silu(c) @ w_mod).reshape(c.shape[0], 6, 1, -1)
    sa, ca, ga, sb, cb, gb = (mod[:, 0].clone(), mod[:, 1].clone(), mod[:, 2].clone(), mod[:, 3].clone(), mod[:, 4].clone(), mod[:, 5].clone())
    h = _layernorm(x, axis=-1) * (1 + ca) + sa
    x = x + ga * _A_ref(h, A_k, A_v, A_shift_k, A_ssm_k, A_ssm_D)
    h = _layernorm(x, axis=-1) * (1 + cb) + sb
    x = x + gb * _B_ref(h, B_w_gate, B_w_up, B_w_down)
    return x
```

```cpp
#include <hip/hip_runtime.h>
#include <hip/hip_bf16.h>

typedef unsigned int u32;
typedef u32 u32x2 __attribute__((ext_vector_type(2)));
typedef u32 u32x4 __attribute__((ext_vector_type(4)));
typedef float f32x2 __attribute__((ext_vector_type(2)));
typedef float f32x4 __attribute__((ext_vector_type(4)));
typedef float f32x16 __attribute__((ext_vector_type(16)));
typedef __bf16 bf16x2 __attribute__((ext_vector_type(2)));
typedef __bf16 bf16x8 __attribute__((ext_vector_type(8)));

#define DEV __device__ __forceinline__

constexpr int S_ = 2048, D_ = 448, F_ = 1792;
constexpr float EPS_ = 1e-5f;

constexpr size_t WS_MOD = 0;
constexpr size_t WS_STAT = 16384;
constexpr size_t WS_C12 = 32768;
constexpr size_t WS_UX = 65536;
constexpr size_t WS_X1F = WS_UX + (size_t)256 * 448 * 32;
constexpr size_t WS_WGU = WS_X1F + (size_t)128 * 14 * 1024;
constexpr size_t WS_WD = WS_WGU + (size_t)112 * 2 * 14 * 1024;
constexpr size_t WS_ACT = WS_WD + (size_t)14 * 112 * 1024;
constexpr size_t WS_END = WS_ACT + (size_t)64 * 112 * 1024;

DEV float bflo(u32 v) { return __builtin_bit_cast(float, v << 16); }
DEV float bfhi(u32 v) { return __builtin_bit_cast(float, v & 0xffff0000u); }
DEV u32 pk2(float a, float b) {
  f32x2 v = {a, b};
  bf16x2 r = __builtin_convertvector(v, bf16x2);
  return __builtin_bit_cast(u32, r);
}
DEV f32x2 up2(u32 v) { return f32x2{bflo(v), bfhi(v)}; }
DEV f32x2 rb2(f32x2 v) { return up2(pk2(v.x, v.y)); }
DEV float rb1(float a) { return bflo(pk2(a, a)); }
DEV float bf2f(unsigned short b) { return __builtin_bit_cast(float, (u32)b << 16); }

DEV f32x4 mfma16(u32x4 a, u32x4 b, f32x4 c) {
  return __builtin_amdgcn_mfma_f32_16x16x32_bf16(__builtin_bit_cast(bf16x8, a), __builtin_bit_cast(bf16x8, b), c, 0, 0, 0);
}
DEV f32x16 mfma32(u32x4 a, u32x4 b, f32x16 c) {
  return __builtin_amdgcn_mfma_f32_32x32x16_bf16(__builtin_bit_cast(bf16x8, a), __builtin_bit_cast(bf16x8, b), c, 0, 0, 0);
}
DEV float dot2(u32 a, u32 b, float c) {
  return __builtin_amdgcn_fdot2_f32_bf16(__builtin_bit_cast(bf16x2, a), __builtin_bit_cast(bf16x2, b), c, false);
}
template <int CTRL> DEV float dppf(float v) {
  return __builtin_bit_cast(float, __builtin_amdgcn_update_dpp(0, __builtin_bit_cast(int, v), CTRL, 0xf, 0xf, true));
}
DEV float row16_sum(float v) {
  v += dppf<0xB1>(v);
  v += dppf<0x4E>(v);
  v += dppf<0x141>(v);
  v += dppf<0x140>(v);
  return v;
}
DEV float rdlane(float v, int l) {
  return __builtin_bit_cast(float, __builtin_amdgcn_readlane(__builtin_bit_cast(int, v), l));
}
DEV float wave_sum(float v) {
  v = row16_sum(v);
  return (rdlane(v, 0) + rdlane(v, 16)) + (rdlane(v, 32) + rdlane(v, 48));
}

constexpr int K1_TR = 256;
constexpr int K1_ST = 128;
constexpr int K1_MOD = 84;

__global__ void __launch_bounds__(256) k1_prep(const u32* __restrict__ xq, const u32* __restrict__ kin,
                                               const u32* __restrict__ vin, const u32x4* __restrict__ shiftk,
                                               const unsigned short* __restrict__ cvec,
                                               const u32x4* __restrict__ wmod, unsigned char* __restrict__ ws) {
  __shared__ float red[64 * 33];
  const int tid = threadIdx.x;
  const int b = blockIdx.x;
  if (b < K1_MOD) {
    const int cq = tid & 3, rg = tid >> 2;
    u32x4 w[7];
    float sc[7];
#pragma unroll
    for (int i = 0; i < 7; ++i) w[i] = wmod[(size_t)(rg + 64 * i) * 336 + b * 4 + cq];
#pragma unroll
    for (int i = 0; i < 7; ++i) {
      float c = bf2f(cvec[rg + 64 * i]);
      sc[i] = rb1(c / (1.0f + __expf(-c)));
    }
    float acc[8];
#pragma unroll
    for (int j = 0; j < 8; ++j) acc[j] = 0.f;
#pragma unroll
    for (int i = 0; i < 7; ++i) {
#pragma unroll
      for (int j = 0; j < 4; ++j) {
        acc[2 * j] = fmaf(sc[i], bflo(w[i][j]), acc[2 * j]);
        acc[2 * j + 1] = fmaf(sc[i], bfhi(w[i][j]), acc[2 * j + 1]);
      }
    }
#pragma unroll
    for (int j = 0; j < 8; ++j) red[rg * 33 + cq * 8 + j] = acc[j];
    __syncthreads();
    const int col = tid >> 3, part = tid & 7;
    float s = 0.f;
#pragma unroll
    for (int i = 0; i < 8; ++i) s += red[(part * 8 + i) * 33 + col];
    s += dppf<0xB1>(s);
    s += dppf<0x4E>(s);
    s += dppf<0x141>(s);
    if (part == 0) ((float*)(ws + WS_MOD))[b * 32 + col] = rb1(s);
    return;
  }
  if (b < K1_MOD + K1_TR) {
    const int c = b - K1_MOD;
    if (tid >= 224) return;
    const int t0 = c * 8;
    u32 kk[11], vv[8], xx[8];
#pragma unroll
    for (int r = 0; r < 11; ++r) {
      int t = t0 - 3 + r;
      kk[r] = (t >= 0) ? kin[(size_t)t * 224 + tid] : 0u;
    }
#pragma unroll
    for (int r = 0; r < 8; ++r) vv[r] = vin[(size_t)(t0 + r) * 224 + tid];
#pragma unroll
    for (int r = 0; r < 8; ++r) xx[r] = xq[(size_t)(t0 + r) * 224 + tid];
    const u32x4 sw = shiftk[tid];
    f32x2 w0 = {bflo(sw.x), bflo(sw.z)}, w1 = {bfhi(sw.x), bfhi(sw.z)};
    f32x2 w2 = {bflo(sw.y), bflo(sw.w)}, w3 = {bfhi(sw.y), bfhi(sw.w)};
    f32x2 kf[11];
#pragma unroll
    for (int r = 0; r < 11; ++r) kf[r] = up2(kk[r]);
    u32 kv[8];
#pragma unroll
    for (int r = 0; r < 8; ++r) {
      f32x2 a = rb2(kf[r] * w3);
      a = rb2(a + rb2(kf[r + 1] * w2));
      a = rb2(a + rb2(kf[r + 2] * w1));
      a = rb2(a + rb2(kf[r + 3] * w0));
      f32x2 p = a * up2(vv[r]);
      kv[r] = pk2(p.x, p.y);
    }
    u32x4 u0, u1, x0, x1;
#pragma unroll
    for (int i = 0; i < 4; ++i) {
      u0[i] = __builtin_amdgcn_perm(kv[6 - 2 * i], kv[7 - 2 * i], 0x05040100u);
      u1[i] = __builtin_amdgcn_perm(kv[6 - 2 * i], kv[7 - 2 * i], 0x07060302u);
      x0[i] = __builtin_amdgcn_perm(xx[2 * i + 1], xx[2 * i], 0x05040100u);
      x1[i] = __builtin_amdgcn_perm(xx[2 * i + 1], xx[2 * i], 0x07060302u);
    }
    u32x4* dst = (u32x4*)(ws + WS_UX) + ((size_t)c * 448 + 2 * tid) * 2;
    dst[0] = u0;
    dst[1] = x0;
    dst[2] = u1;
    dst[3] = x1;
    return;
  }
  {
    const int sb = b - K1_MOD - K1_TR;
    const int lane = tid & 63, w = tid >> 6;
    const int r0 = sb * 16 + w * 4;
    u32x4 xv[4];
#pragma unroll
    for (int r = 0; r < 4; ++r)
      xv[r] = (lane < 56) ? ((const u32x4*)xq)[(size_t)(r0 + r) * 56 + lane] : u32x4{0u, 0u, 0u, 0u};
#pragma unroll
    for (int r = 0; r < 4; ++r) {
      float s = 0.f;
#pragma unroll
      for (int j = 0; j < 4; ++j) s += bflo(xv[r][j]) + bfhi(xv[r][j]);
      const float mean = wave_sum(s) * (1.0f / D_);
      float q = 0.f;
#pragma unroll
      for (int j = 0; j < 4; ++j) {
        float a = bflo(xv[r][j]) - mean, bb = bfhi(xv[r][j]) - mean;
        q = fmaf(a, a, q);
        q = fmaf(bb, bb, q);
      }
      if (lane >= 56) q = 0.f;
      const float var = wave_sum(q) * (1.0f / D_);
      const float sd = rb1(sqrtf(rb1(rb1(var) + EPS_)));
      if (lane == 0) ((f32x2*)(ws + WS_STAT))[r0 + r] = f32x2{rb1(mean), 1.0f / sd};
    }
  }
}

constexpr int CV_CS = 4160;
constexpr int CV_UOFF = 8 * CV_CS;
constexpr int CV_CH = CV_UOFF + 286 * 16 + 32;
constexpr int K2_LDS = 2 * CV_CH;
constexpr int K2_CONV = 224, K2_PREP = 224;

__global__ void __launch_bounds__(256, 2) k2_conv(const u32x4* __restrict__ ssmk, const unsigned short* __restrict__ ssmD,
                                                  const u32x2* __restrict__ wgate, const u32x2* __restrict__ wup,
                                                  unsigned char* __restrict__ ws) {
  extern __shared__ __attribute__((aligned(16))) unsigned char smem[];
  const int tid = threadIdx.x;
  const int b = blockIdx.x;
  const float* __restrict__ mod = (const float*)(ws + WS_MOD);
  if (b >= K2_CONV) {
    const int pb = b - K2_CONV;
    const int xcd = pb & 7, idx = pb >> 3;
    const int unit = xcd * 28 + idx;
    const int mat = unit / 112, ft = unit % 112;
    const u32x2* __restrict__ W = mat ? wup : wgate;
    float* red = (float*)smem;
    const int fq = tid & 3, ko = tid >> 2;
    float c1[4] = {0.f, 0.f, 0.f, 0.f}, c2[4] = {0.f, 0.f, 0.f, 0.f};
    if (ko < 56) {
      u32x2 w[8];
#pragma unroll
      for (int j = 0; j < 8; ++j) w[j] = W[(size_t)(ko * 8 + j) * 448 + ft * 4 + fq];
      const f32x4 cbA = *(const f32x4*)(mod + 4 * D_ + ko * 8), cbB = *(const f32x4*)(mod + 4 * D_ + ko * 8 + 4);
      const f32x4 sbA = *(const f32x4*)(mod + 3 * D_ + ko * 8), sbB = *(const f32x4*)(mod + 3 * D_ + ko * 8 + 4);
      float sc[8], sb[8];
#pragma unroll
      for (int j = 0; j < 4; ++j) {
        sc[j] = rb1(1.0f + cbA[j]);
        sc[j + 4] = rb1(1.0f + cbB[j]);
        sb[j] = sbA[j];
        sb[j + 4] = sbB[j];
      }
      u32x4 o[4];
#pragma unroll
      for (int jj = 0; jj < 4; ++jj) {
        float a[4] = {bflo(w[2 * jj].x), bfhi(w[2 * jj].x), bflo(w[2 * jj].y), bfhi(w[2 * jj].y)};
        float bb[4] = {bflo(w[2 * jj + 1].x), bfhi(w[2 * jj + 1].x), bflo(w[2 * jj + 1].y), bfhi(w[2 * jj + 1].y)};
#pragma unroll
        for (int f = 0; f < 4; ++f) {
          u32 p = pk2(a[f] * sc[2 * jj], bb[f] * sc[2 * jj + 1]);
          o[f][jj] = p;
          c1[f] += bflo(p) + bfhi(p);
          c2[f] = fmaf(a[f], sb[2 * jj], c2[f]);
          c2[f] = fmaf(bb[f], sb[2 * jj + 1], c2[f]);
        }
      }
      u32x4* dst = (u32x4*)(ws + WS_WGU) + (((size_t)ft * 2 + mat) * 14 + (ko >> 2)) * 64 + (ko & 3) * 16 + fq * 4;
#pragma unroll
      for (int f = 0; f < 4; ++f) dst[f] = o[f];
    }
#pragma unroll
    for (int f = 0; f < 4; ++f) {
      red[ko * 33 + fq * 4 + f] = c1[f];
      red[ko * 33 + 16 + fq * 4 + f] = c2[f];
    }
    __syncthreads();
    const int col = tid >> 3, part = tid & 7;
    float s = 0.f;
#pragma unroll
    for (int i = 0; i < 8; ++i) s += red[(part * 8 + i) * 33 + col];
    s += dppf<0xB1>(s);
    s += dppf<0x4E>(s);
    s += dppf<0x141>(s);
    if (part == 0) ((float*)(ws + WS_C12))[(mat * 2 + (col >> 4)) * F_ + ft * 16 + (col & 15)] = s;
    return;
  }

  const int pair = (b & 7) * 28 + (b >> 3);
  const int d0 = 2 * pair;
  const int lane = tid & 63;
  const int w = __builtin_amdgcn_readfirstlane(tid >> 6);
  const int ch = w >> 1;
  const int i = tid & 127;
  unsigned char* cb = smem + ch * CV_CH;
  const u32x4* __restrict__ ux = (const u32x4*)(ws + WS_UX);

  const u32x4* kr = ssmk + (size_t)(d0 + ch) * 256;
  u32x4 ka[2], kb[2], uu[2];
  ka[0] = (i > 0) ? kr[i - 1] : u32x4{0u, 0u, 0u, 0u};
  kb[0] = kr[i];
  ka[1] = kr[i + 127];
  kb[1] = kr[i + 128 < 256 ? i + 128 : 255];
  uu[0] = ux[((size_t)i * 448 + d0 + ch) * 2];
  uu[1] = ux[((size_t)(i + 128) * 448 + d0 + ch) * 2];
  const int n = lane & 15, g = lane >> 4;
  u32x2 xe[2][2];
  f32x4 st[2][2];
#pragma unroll
  for (int c2 = 0; c2 < 2; ++c2) {
    const int t0 = 256 * (2 * w + c2) + 16 * n + 4 * g;
#pragma unroll
    for (int cc = 0; cc < 2; ++cc)
      xe[c2][cc] = ((const u32x2*)(ux + ((size_t)(t0 >> 3) * 448 + d0 + cc) * 2 + 1))[g & 1];
    st[c2][0] = ((const f32x4*)(ws + WS_STAT))[t0 / 2];
    st[c2][1] = ((const f32x4*)(ws + WS_STAT))[t0 / 2 + 1];
  }
  if (i < 8) *(u32x4*)(cb + i * CV_CS) = u32x4{0u, 0u, 0u, 0u};
  else if (i < 38) *(u32x4*)(cb + CV_UOFF + (256 + i - 8) * 16) = u32x4{0u, 0u, 0u, 0u};
#pragma unroll
  for (int it = 0; it < 2; ++it) {
    const int qp = i + 1 + 128 * it;
    u32 Dw[8] = {ka[it].x, ka[it].y, ka[it].z, ka[it].w, kb[it].x, kb[it].y, kb[it].z, kb[it].w};
    u32 E[7];
#pragma unroll
    for (int j = 0; j < 7; ++j) E[j] = __builtin_amdgcn_alignbit(Dw[j + 1], Dw[j], 16);
    unsigned char* p = cb + qp * 16;
#pragma unroll
    for (int s2 = 0; s2 < 4; ++s2) {
      *(u32x4*)(p + (2 * s2) * CV_CS) = u32x4{E[s2], E[s2 + 1], E[s2 + 2], E[s2 + 3]};
      *(u32x4*)(p + (2 * s2 + 1) * CV_CS) = u32x4{Dw[s2 + 1], Dw[s2 + 2], Dw[s2 + 3], Dw[s2 + 4]};
    }
    *(u32x4*)(cb + CV_UOFF + (255 - (i + 128 * it)) * 16) = uu[it];
  }
  __syncthreads();

  const int sh = w & 1;
  const unsigned char* ap = cb + (n & 7) * CV_CS + ((n >> 3) + g) * 16 + sh * 256;
  const unsigned char* bp = cb + CV_UOFF + (254 - 2 * n + g) * 16 - 3584 + sh * 256;
  f32x4 acc[8];
#pragma unroll
  for (int c = 0; c < 8; ++c) acc[c] = f32x4{0.f, 0.f, 0.f, 0.f};
#pragma unroll
  for (int s = 0; s < 4; ++s) {
    u32x4 A[8], B[8];
#pragma unroll
    for (int e = 0; e < 8; ++e) {
      A[e] = *(const u32x4*)(ap + 64 * s + 512 * e);
      B[e] = *(const u32x4*)(bp + 64 * s + 512 * (7 - e));
    }
#pragma unroll
    for (int c = 0; c < 8; ++c)
#pragma unroll
      for (int ii = 0; ii <= c; ++ii) acc[c] = mfma16(A[ii], B[c - ii], acc[c]);
  }
  __syncthreads();
  f32x4* red = (f32x4*)smem;
#pragma unroll
  for (int c = 0; c < 8; ++c) red[(w * 8 + c) * 64 + lane] = acc[c];
  __syncthreads();

  const float ca0 = rb1(1.0f + mod[D_ + d0]), ca1 = rb1(1.0f + mod[D_ + d0 + 1]);
  const f32x2 CA = {ca0, ca1};
  const f32x2 SA = {mod[d0], mod[d0 + 1]};
  const f32x2 GA = {mod[2 * D_ + d0], mod[2 * D_ + d0 + 1]};
  const f32x2 DD = {bf2f(ssmD[d0]), bf2f(ssmD[d0 + 1])};
  u32* x1f = (u32*)(ws + WS_X1F);
  const int ks = d0 >> 5, gk = (d0 >> 3) & 3, jj = (d0 & 7) >> 1;
#pragma unroll
  for (int c2 = 0; c2 < 2; ++c2) {
    const int ct = 2 * w + c2;
    f32x4 y0 = red[(0 * 8 + ct) * 64 + lane] + red[(1 * 8 + ct) * 64 + lane];
    f32x4 y1 = red[(2 * 8 + ct) * 64 + lane] + red[(3 * 8 + ct) * 64 + lane];
    const int t0 = 256 * ct + 16 * n + 4 * g;
    const int uo = CV_UOFF + (255 - (t0 >> 3)) * 16 + (1 - (g & 1)) * 8;
    const u32x2 ur0 = *(const u32x2*)(smem + uo);
    const u32x2 ur1 = *(const u32x2*)(smem + CV_CH + uo);
    const float u0[4] = {bfhi(ur0.y), bflo(ur0.y), bfhi(ur0.x), bflo(ur0.x)};
    const float u1[4] = {bfhi(ur1.y), bflo(ur1.y), bfhi(ur1.x), bflo(ur1.x)};
    const float xa[4] = {bflo(xe[c2][0].x), bfhi(xe[c2][0].x), bflo(xe[c2][0].y), bfhi(xe[c2][0].y)};
    const float xb[4] = {bflo(xe[c2][1].x), bfhi(xe[c2][1].x), bflo(xe[c2][1].y), bfhi(xe[c2][1].y)};
    const float mn[4] = {st[c2][0].x, st[c2][0].z, st[c2][1].x, st[c2][1].z};
    const float rs[4] = {st[c2][0].y, st[c2][0].w, st[c2][1].y, st[c2][1].w};
#pragma unroll
    for (int r = 0; r < 4; ++r) {
      const f32x2 X = {xa[r], xb[r]};
      const f32x2 Y = rb2(f32x2{y0[r], y1[r]});
      const f32x2 Z = rb2(Y + rb2(f32x2{u0[r], u1[r]} * DD));
      const f32x2 XN = rb2(rb2(X - mn[r]) * rs[r]);
      const f32x2 H = rb2(rb2(XN * CA) + SA);
      const f32x2 R = X + rb2(GA * rb2(H * Z));
      const int tile = 16 * ct + 4 * g + r;
      x1f[(((size_t)tile * 14 + ks) * 64 + gk * 16 + n) * 4 + jj] = pk2(R.x, R.y);
    }
  }
}

constexpr int K3_XLDS = 8 * 14 * 1024;
constexpr int K3_LDS = K3_XLDS + 1024;

DEV float gelu_tanh(float x) {
  const float k0 = -2.0f * 0.7978845608028654f * 1.4426950408889634f;
  const float k1 = k0 * 0.044715f;
  const float e = __builtin_amdgcn_exp2f(x * fmaf(x * x, k1, k0));
  return x * __builtin_amdgcn_rcpf(1.0f + e);
}

__global__ void __launch_bounds__(512, 2) k3_gemm1(const u32* __restrict__ wdown, unsigned char* __restrict__ ws) {
  extern __shared__ __attribute__((aligned(16))) unsigned char smem[];
  const int tid = threadIdx.x;
  const int b = blockIdx.x;
  const int xcd = b & 7, idx = b >> 3;
  if (idx >= 28) {
    const int pb = xcd * 4 + (idx - 28);
    u32x4* dst = (u32x4*)(ws + WS_WD);
#pragma unroll
    for (int it = 0; it < 4; ++it) {
      const int li = it * 512 + tid;
      if (li < 1568) {
        const int item = pb * 1568 + li;
        const int fo = item / 224, np = item - fo * 224;
        u32 wv[8];
#pragma unroll
        for (int j = 0; j < 8; ++j) wv[j] = wdown[(size_t)(fo * 8 + j) * 224 + np];
        u32x4 o0, o1;
#pragma unroll
        for (int j = 0; j < 4; ++j) {
          o0[j] = __builtin_amdgcn_perm(wv[2 * j + 1], wv[2 * j], 0x05040100u);
          o1[j] = __builtin_amdgcn_perm(wv[2 * j + 1], wv[2 * j], 0x07060302u);
        }
        const int nn = 2 * np;
        u32x4* p = dst + ((size_t)(nn >> 5) * 112 + (fo >> 1)) * 64 + (fo & 1) * 32 + (nn & 31);
        p[0] = o0;
        p[1] = o1;
      }
    }
    return;
  }
  const int Mt = 2 * xcd + idx / 14, Fs = idx % 14;
  const int lane = tid & 63;
  const int w = __builtin_amdgcn_readfirstlane(tid >> 6);
  const int ft = Fs * 8 + w;
  const u32x4* __restrict__ xsrc = (const u32x4*)(ws + WS_X1F) + (size_t)(Mt * 8 + w) * 14 * 64 + lane;
  const u32x4* __restrict__ wsrc = (const u32x4*)(ws + WS_WGU) + (size_t)ft * 28 * 64 + lane;
  u32x4* xl = (u32x4*)smem;
  f32x2* stl = (f32x2*)(smem + K3_XLDS);

  u32x4 xs[14], wg[14], wu[14];
#pragma unroll
  for (int k = 0; k < 7; ++k) xs[k] = xsrc[k * 64];
#pragma unroll
  for (int k = 0; k < 7; ++k) {
    wg[k] = wsrc[k * 64];
    wu[k] = wsrc[(14 + k) * 64];
  }
#pragma unroll
  for (int k = 7; k < 14; ++k) xs[k] = xsrc[k * 64];
#pragma unroll
  for (int k = 7; k < 10; ++k) {
    wg[k] = wsrc[k * 64];
    wu[k] = wsrc[(14 + k) * 64];
  }
  __builtin_amdgcn_sched_barrier(0);
  float s1 = 0.f, s2 = 0.f;
#pragma unroll
  for (int k = 0; k < 7; ++k) {
    xl[(k * 8 + w) * 64 + lane] = xs[k];
#pragma unroll
    for (int j = 0; j < 4; ++j) {
      s1 = dot2(xs[k][j], 0x3f803f80u, s1);
      s2 = dot2(xs[k][j], xs[k][j], s2);
    }
  }
  __builtin_amdgcn_sched_barrier(0);
#pragma unroll
  for (int k = 10; k < 14; ++k) {
    wg[k] = wsrc[k * 64];
    wu[k] = wsrc[(14 + k) * 64];
  }
  __builtin_amdgcn_sched_barrier(0);
  __syncthreads();

  f32x4 ag[8], au[8];
#pragma unroll
  for (int m = 0; m < 8; ++m) {
    ag[m] = f32x4{0.f, 0.f, 0.f, 0.f};
    au[m] = f32x4{0.f, 0.f, 0.f, 0.f};
  }
#pragma unroll
  for (int k = 0; k < 7; ++k) {
#pragma unroll
    for (int m = 0; m < 8; ++m) {
      const u32x4 xf = xl[(k * 8 + m) * 64 + lane];
      ag[m] = mfma16(wg[k], xf, ag[m]);
      au[m] = mfma16(wu[k], xf, au[m]);
    }
    if (k == 3) {
#pragma unroll
      for (int kk = 7; kk < 14; ++kk) {
        xl[(kk * 8 + w) * 64 + lane] = xs[kk];
#pragma unroll
        for (int j = 0; j < 4; ++j) {
          s1 = dot2(xs[kk][j], 0x3f803f80u, s1);
          s2 = dot2(xs[kk][j], xs[kk][j], s2);
        }
      }
      s1 += __shfl_xor(s1, 16);
      s2 += __shfl_xor(s2, 16);
      s1 += __shfl_xor(s1, 32);
      s2 += __shfl_xor(s2, 32);
      const float mean = s1 * (1.0f / D_);
      const float var = fmaxf(s2 * (1.0f / D_) - mean * mean, 0.f);
      const float sd = rb1(sqrtf(rb1(rb1(var) + EPS_)));
      if (lane < 16) stl[w * 16 + lane] = f32x2{rb1(mean), 1.0f / sd};
    }
  }
  __syncthreads();
  const int g = lane >> 4, m16 = lane & 15;
  const float* c12 = (const float*)(ws + WS_C12) + ft * 16 + 4 * g;
  const f32x4 c1g = *(const f32x4*)(c12), c2g = *(const f32x4*)(c12 + F_);
  const f32x4 c1u = *(const f32x4*)(c12 + 2 * F_), c2u = *(const f32x4*)(c12 + 3 * F_);
#pragma unroll
  for (int k = 7; k < 14; ++k) {
#pragma unroll
    for (int m = 0; m < 8; ++m) {
      const u32x4 xf = xl[(k * 8 + m) * 64 + lane];
      ag[m] = mfma16(wg[k], xf, ag[m]);
      au[m] = mfma16(wu[k], xf, au[m]);
    }
  }
  u32x2* act = (u32x2*)(ws + WS_ACT);
#pragma unroll
  for (int m = 0; m < 8; ++m) {
    const f32x2 stt = stl[m * 16 + m16];
    float o[4];
#pragma unroll
    for (int r = 0; r < 4; ++r) {
      const float gt = rb1(fmaf(fmaf(-stt.x, c1g[r], ag[m][r]), stt.y, c2g[r]));
      const float ut = rb1(fmaf(fmaf(-stt.x, c1u[r], au[m][r]), stt.y, c2u[r]));
      o[r] = rb1(gelu_tanh(gt)) * ut;
    }
    const int mt32 = Mt * 4 + (m >> 1);
    const int lp = (g >> 1) * 32 + (m & 1) * 16 + m16;
    act[(((size_t)mt32 * 112 + ft) * 64 + lp) * 2 + (g & 1)] = u32x2{pk2(o[0], o[1]), pk2(o[2], o[3])};
  }
}

constexpr int K4_LDS = 4 * 4 * 4 * 1024;
constexpr int K4_DEPTH = 12;

__global__ void __launch_bounds__(256, 1) k4_gemm2(unsigned char* __restrict__ ws, u32x2* __restrict__ out) {
  extern __shared__ __attribute__((aligned(16))) unsigned char smem[];
  const int tid = threadIdx.x;
  const int b = blockIdx.x;
  const int xcd = b & 7, idx = b >> 3;
  const int mt64 = 4 * xcd + idx / 7, nt64 = idx % 7;
  const int lane = tid & 63;
  const int w = __builtin_amdgcn_readfirstlane(tid >> 6);
  const u32x4* __restrict__ asrc = (const u32x4*)(ws + WS_ACT) + ((size_t)(mt64 * 2) * 112 + 28 * w) * 64 + lane;
  const u32x4* __restrict__ wsrc = (const u32x4*)(ws + WS_WD) + ((size_t)(nt64 * 2) * 112 + 28 * w) * 64 + lane;

  u32x4 ra[K4_DEPTH][2], rw[K4_DEPTH][2];
#pragma unroll
  for (int k = 0; k < K4_DEPTH; ++k) {
    ra[k][0] = asrc[k * 64];
    rw[k][0] = wsrc[k * 64];
    ra[k][1] = asrc[(112 + k) * 64];
    rw[k][1] = wsrc[(112 + k) * 64];
  }
  const int m32 = lane & 31, h = lane >> 5;
  const int mu = mt64 * 64 + (w & 1) * 32 + m32;
  const int nb = nt64 * 64 + (w >> 1) * 32 + 4 * h;
  u32x2 xr[4];
  f32x4 gb[4];
#pragma unroll
  for (int q = 0; q < 4; ++q) {
    const int nn = nb + 8 * q;
    xr[q] = ((const u32x2*)(ws + WS_X1F))[((((size_t)(mu >> 4) * 14 + (nn >> 5)) * 64 + ((nn >> 3) & 3) * 16 + (mu & 15)) << 1) + h];
    gb[q] = *(const f32x4*)((const float*)(ws + WS_MOD) + 5 * D_ + nn);
  }
  __builtin_amdgcn_sched_barrier(0);

  f32x16 acc[2][2];
#pragma unroll
  for (int a = 0; a < 2; ++a)
#pragma unroll
    for (int c = 0; c < 2; ++c)
#pragma unroll
      for (int r = 0; r < 16; ++r) acc[a][c][r] = 0.f;
#pragma unroll
  for (int k = 0; k < 28; ++k) {
    const int sl = k % K4_DEPTH;
    const u32x4 a0 = ra[sl][0], a1 = ra[sl][1], w0 = rw[sl][0], w1 = rw[sl][1];
    acc[0][0] = mfma32(w0, a0, acc[0][0]);
    acc[0][1] = mfma32(w0, a1, acc[0][1]);
    acc[1][0] = mfma32(w1, a0, acc[1][0]);
    acc[1][1] = mfma32(w1, a1, acc[1][1]);
    if (k + K4_DEPTH < 28) {
      ra[sl][0] = asrc[(k + K4_DEPTH) * 64];
      rw[sl][0] = wsrc[(k + K4_DEPTH) * 64];
      ra[sl][1] = asrc[(112 + k + K4_DEPTH) * 64];
      rw[sl][1] = wsrc[(112 + k + K4_DEPTH) * 64];
    }
    __builtin_amdgcn_sched_barrier(0);
  }
  f32x4* red = (f32x4*)smem;
#pragma unroll
  for (int tl = 0; tl < 4; ++tl) {
#pragma unroll
    for (int q = 0; q < 4; ++q) {
      const f32x16& A = acc[tl >> 1][tl & 1];
      red[((w * 4 + tl) * 4 + q) * 64 + lane] = f32x4{A[4 * q], A[4 * q + 1], A[4 * q + 2], A[4 * q + 3]};
    }
  }
  __syncthreads();
  const int t = (mu & ~255) | ((mu & 15) << 4) | ((mu >> 4) & 15);
#pragma unroll
  for (int q = 0; q < 4; ++q) {
    f32x4 s = (red[((0 * 4 + w) * 4 + q) * 64 + lane] + red[((1 * 4 + w) * 4 + q) * 64 + lane]) +
              (red[((2 * 4 + w) * 4 + q) * 64 + lane] + red[((3 * 4 + w) * 4 + q) * 64 + lane]);
    const float xv[4] = {bflo(xr[q].x), bfhi(xr[q].x), bflo(xr[q].y), bfhi(xr[q].y)};
    float o[4];
#pragma unroll
    for (int r = 0; r < 4; ++r) o[r] = xv[r] + rb1(gb[q][r] * rb1(s[r]));
    out[((size_t)t * D_ + nb + 8 * q) >> 2] = u32x2{pk2(o[0], o[1]), pk2(o[2], o[3])};
  }
}

extern "C" void kernel_launch(void* const* d_in, const int* in_sizes, int n_in, void* d_out, int out_size, void* d_ws,
                              size_t ws_size, hipStream_t stream) {
  (void)in_sizes; (void)n_in; (void)out_size; (void)ws_size;
  const void* A_q = d_in[0];
  const void* A_k = d_in[1];
  const void* A_v = d_in[2];
  const void* A_shift = d_in[3];
  const void* A_ssmk = d_in[4];
  const void* A_ssmD = d_in[5];
  const void* W_gate = d_in[7];
  const void* W_up = d_in[8];
  const void* W_down = d_in[9];
  const void* cvec = d_in[10];
  const void* w_mod = d_in[11];
  unsigned char* ws = (unsigned char*)d_ws;

  (void)hipFuncSetAttribute((const void*)k2_conv, hipFuncAttributeMaxDynamicSharedMemorySize, K2_LDS);
  (void)hipFuncSetAttribute((const void*)k3_gemm1, hipFuncAttributeMaxDynamicSharedMemorySize, K3_LDS);
  (void)hipFuncSetAttribute((const void*)k4_gemm2, hipFuncAttributeMaxDynamicSharedMemorySize, K4_LDS);

  k1_prep<<<K1_MOD + K1_TR + K1_ST, 256, 0, stream>>>((const u32*)A_q, (const u32*)A_k, (const u32*)A_v,
                                                       (const u32x4*)A_shift, (const unsigned short*)cvec,
                                                       (const u32x4*)w_mod, ws);
  k2_conv<<<K2_CONV + K2_PREP, 256, K2_LDS, stream>>>((const u32x4*)A_ssmk, (const unsigned short*)A_ssmD,
                                                       (const u32x2*)W_gate, (const u32x2*)W_up, ws);
  k3_gemm1<<<256, 512, K3_LDS, stream>>>((const u32*)W_down, ws);
  k4_gemm2<<<224, 256, K4_LDS, stream>>>(ws, (u32x2*)d_out);
}
```

```cpp
#include <hip/hip_runtime.h>
#include <hip/hip_bf16.h>

typedef unsigned int u32;
typedef u32 u32x2 __attribute__((ext_vector_type(2)));
typedef u32 u32x4 __attribute__((ext_vector_type(4)));
typedef float f32x2 __attribute__((ext_vector_type(2)));
typedef float f32x4 __attribute__((ext_vector_type(4)));
typedef float f32x16 __attribute__((ext_vector_type(16)));
typedef __bf16 bf16x2 __attribute__((ext_vector_type(2)));
typedef __bf16 bf16x8 __attribute__((ext_vector_type(8)));

#if defined(__HIP_DEVICE_COMPILE__)
#define NOPK __attribute__((target("no-packed-fp32-ops")))
#else
#define NOPK
#endif
#define DEV NOPK __device__ __forceinline__
#define SB() __builtin_amdgcn_sched_barrier(0)

#define STAMP(kid, slot)

constexpr int S_ = 2048, D_ = 448, F_ = 1792;
constexpr float EPS_ = 1e-5f;

constexpr size_t WS_MOD = 0;
constexpr size_t WS_STAT = 16384;
constexpr size_t WS_C12 = 32768;
constexpr size_t WS_FLG = 65536;
constexpr size_t WS_UX = 65536 + 16384;
constexpr size_t WS_X1F = WS_UX + (size_t)224 * 16384;
constexpr size_t WS_WGU = WS_X1F + (size_t)128 * 14 * 1024;
constexpr size_t WS_WD = WS_WGU + (size_t)112 * 2 * 14 * 1024;
constexpr size_t WS_ACT = WS_WD + (size_t)14 * 112 * 1024;
constexpr size_t WS_END = WS_ACT + (size_t)64 * 112 * 1024;

DEV float bflo(u32 v) { return __builtin_bit_cast(float, v << 16); }
DEV float bfhi(u32 v) { return __builtin_bit_cast(float, v & 0xffff0000u); }
DEV u32 pk2(float a, float b) {
  f32x2 v = {a, b};
  bf16x2 r = __builtin_convertvector(v, bf16x2);
  return __builtin_bit_cast(u32, r);
}
DEV f32x2 up2(u32 v) { return f32x2{bflo(v), bfhi(v)}; }
DEV f32x2 rb2(f32x2 v) { return up2(pk2(v.x, v.y)); }
DEV float rb1(float a) { return bflo(pk2(a, a)); }
DEV float bf2f(unsigned short b) { return __builtin_bit_cast(float, (u32)b << 16); }

DEV f32x4 mfma16(u32x4 a, u32x4 b, f32x4 c) {
  return __builtin_amdgcn_mfma_f32_16x16x32_bf16(__builtin_bit_cast(bf16x8, a), __builtin_bit_cast(bf16x8, b), c, 0, 0, 0);
}
DEV f32x16 mfma32(u32x4 a, u32x4 b, f32x16 c) {
  return __builtin_amdgcn_mfma_f32_32x32x16_bf16(__builtin_bit_cast(bf16x8, a), __builtin_bit_cast(bf16x8, b), c, 0, 0, 0);
}
DEV float dot2(u32 a, u32 b, float c) {
  return __builtin_amdgcn_fdot2_f32_bf16(__builtin_bit_cast(bf16x2, a), __builtin_bit_cast(bf16x2, b), c, false);
}
template <int CTRL> DEV float dppf(float v) {
  return __builtin_bit_cast(float, __builtin_amdgcn_update_dpp(0, __builtin_bit_cast(int, v), CTRL, 0xf, 0xf, true));
}
DEV float row16_sum(float v) {
  v += dppf<0xB1>(v);
  v += dppf<0x4E>(v);
  v += dppf<0x141>(v);
  v += dppf<0x140>(v);
  return v;
}
DEV float rdlane(float v, int l) {
  return __builtin_bit_cast(float, __builtin_amdgcn_readlane(__builtin_bit_cast(int, v), l));
}
DEV float wave_sum(float v) {
  v = row16_sum(v);
  return (rdlane(v, 0) + rdlane(v, 16)) + (rdlane(v, 32) + rdlane(v, 48));
}
DEV float col4_sum(float v) {
  u32 a = __builtin_bit_cast(u32, v);
  auto r = __builtin_amdgcn_permlane16_swap(a, a, false, false);
  v = __builtin_bit_cast(float, (u32)r[0]) + __builtin_bit_cast(float, (u32)r[1]);
  a = __builtin_bit_cast(u32, v);
  auto q = __builtin_amdgcn_permlane32_swap(a, a, false, false);
  return __builtin_bit_cast(float, (u32)q[0]) + __builtin_bit_cast(float, (u32)q[1]);
}
DEV void bar() {
  asm volatile("s_waitcnt lgkmcnt(0)" ::: "memory");
  __builtin_amdgcn_s_barrier();
  asm volatile("" ::: "memory");
}
DEV float ln_sd(float var) { return rb1(__builtin_amdgcn_sqrtf(rb1(rb1(var) + EPS_))); }
DEV float ln_rsd(float var) { return __builtin_amdgcn_rcpf(ln_sd(var)); }
DEV void rows_bcast(float v, float (&o)[4]) {
  const u32 a = __builtin_bit_cast(u32, v);
  auto r = __builtin_amdgcn_permlane16_swap(a, a, false, false);
  auto e = __builtin_amdgcn_permlane32_swap((u32)r[0], (u32)r[0], false, false);
  auto d = __builtin_amdgcn_permlane32_swap((u32)r[1], (u32)r[1], false, false);
  o[0] = __builtin_bit_cast(float, (u32)e[0]);
  o[2] = __builtin_bit_cast(float, (u32)e[1]);
  o[1] = __builtin_bit_cast(float, (u32)d[0]);
  o[3] = __builtin_bit_cast(float, (u32)d[1]);
}

constexpr int K1_TR = 256;
constexpr int K1_ST = 128;
constexpr int K1_MOD = 84;

NOPK __global__ void __launch_bounds__(256) k1_prep(const u32* __restrict__ xq, const u32* __restrict__ kin,
                                                    const u32* __restrict__ vin, const u32x4* __restrict__ shiftk,
                                                    const unsigned short* __restrict__ cvec,
                                                    const u32x4* __restrict__ wmod, unsigned char* __restrict__ ws) {
  __shared__ float red[64 * 33];
  const int tid = threadIdx.x;
  const int b = blockIdx.x;
  STAMP(0, 0);
  if (b >= K1_MOD && b < K1_MOD + K1_TR) {
    if (tid >= 224) return;
    const int rg = (b - K1_MOD) >> 2, cq = (b - K1_MOD) & 3;
    const int p = cq * 56 + (tid >> 2), j = tid & 3;
    const int t0 = rg * 32 + j * 8;
    u32 kk[11], vv[8], xx[8];
#pragma unroll
    for (int r = 0; r < 11; ++r) {
      const int t = t0 - 3 + r;
      kk[r] = kin[(size_t)(t < 0 ? 0 : t) * 224 + p];
    }
#pragma unroll
    for (int r = 0; r < 8; ++r) vv[r] = vin[(size_t)(t0 + r) * 224 + p];
    const u32x4 sw = shiftk[p];
#pragma unroll
    for (int r = 0; r < 8; ++r) xx[r] = xq[(size_t)(t0 + r) * 224 + p];
    SB();
    if (t0 == 0) { kk[0] = 0u; kk[1] = 0u; kk[2] = 0u; }
    const f32x2 w0 = {bflo(sw.x), bflo(sw.z)}, w1 = {bfhi(sw.x), bfhi(sw.z)};
    const f32x2 w2 = {bflo(sw.y), bflo(sw.w)}, w3 = {bfhi(sw.y), bfhi(sw.w)};
    f32x2 kf[11];
#pragma unroll
    for (int r = 0; r < 11; ++r) kf[r] = up2(kk[r]);
    u32 kv[8];
#pragma unroll
    for (int r = 0; r < 8; ++r) {
      float a0 = kf[r].x * w3.x, a1 = kf[r].y * w3.y;
      a0 = __builtin_fmaf(kf[r + 1].x, w2.x, a0); a1 = __builtin_fmaf(kf[r + 1].y, w2.y, a1);
      a0 = __builtin_fmaf(kf[r + 2].x, w1.x, a0); a1 = __builtin_fmaf(kf[r + 2].y, w1.y, a1);
      a0 = __builtin_fmaf(kf[r + 3].x, w0.x, a0); a1 = __builtin_fmaf(kf[r + 3].y, w0.y, a1);
      kv[r] = pk2(a0 * bflo(vv[r]), a1 * bfhi(vv[r]));
    }
    u32x4 u0, u1, x0, x1;
#pragma unroll
    for (int i = 0; i < 4; ++i) {
      u0[i] = __builtin_amdgcn_perm(kv[6 - 2 * i], kv[7 - 2 * i], 0x05040100u);
      u1[i] = __builtin_amdgcn_perm(kv[6 - 2 * i], kv[7 - 2 * i], 0x07060302u);
      x0[i] = __builtin_amdgcn_perm(xx[2 * i + 1], xx[2 * i], 0x05040100u);
      x1[i] = __builtin_amdgcn_perm(xx[2 * i + 1], xx[2 * i], 0x07060302u);
    }
    u32x4* dst = (u32x4*)(ws + WS_UX) + ((size_t)p * 64 + rg) * 16 + j;
    dst[0] = u0;
    dst[4] = x0;
    dst[8] = u1;
    dst[12] = x1;
    STAMP(0, 2);
    return;
  }
  if (b < K1_MOD) {
    const int mb = b;
    const int cq = tid & 3, rg = tid >> 2;
    u32x4 w[7];
    unsigned short cs[7];
#pragma unroll
    for (int i = 0; i < 7; ++i) cs[i] = cvec[rg + 64 * i];
    SB();
#pragma unroll
    for (int i = 0; i < 7; ++i) w[i] = wmod[(size_t)(rg + 64 * i) * 336 + mb * 4 + cq];
    SB();
    float scv[7];
#pragma unroll
    for (int i = 0; i < 7; ++i) {
      const float c = bf2f(cs[i]);
      scv[i] = rb1(c * __builtin_amdgcn_rcpf(1.0f + __builtin_amdgcn_exp2f(-1.4426950408889634f * c)));
    }
    SB();
    float acc[8];
#pragma unroll
    for (int j = 0; j < 8; ++j) acc[j] = 0.f;
#pragma unroll
    for (int i = 0; i < 7; ++i) {
      const float sc = scv[i];
#pragma unroll
      for (int j = 0; j < 4; ++j) {
        acc[2 * j] = __builtin_fmaf(sc, bflo(w[i][j]), acc[2 * j]);
        acc[2 * j + 1] = __builtin_fmaf(sc, bfhi(w[i][j]), acc[2 * j + 1]);
      }
    }
#pragma unroll
    for (int j = 0; j < 8; ++j) red[rg * 33 + cq * 8 + j] = acc[j];
    bar();
    const int col = tid >> 3, part = tid & 7;
    float s = 0.f;
#pragma unroll
    for (int i = 0; i < 8; ++i) s += red[(part * 8 + i) * 33 + col];
    s += dppf<0xB1>(s);
    s += dppf<0x4E>(s);
    s += dppf<0x141>(s);
    if (part == 0) ((float*)(ws + WS_MOD))[mb * 32 + col] = rb1(s);
    STAMP(0, 1);
    return;
  }
  {
    const int sb = b - K1_MOD - K1_TR;
    const int lane = tid & 63, w = tid >> 6;
    const int r0 = sb * 16 + w * 4;
    const int lc = lane < 56 ? lane : 55;
    if (tid < 32) ((u32*)(ws + WS_FLG))[sb * 32 + tid] = 0u;
    u32x4 xv[4];
#pragma unroll
    for (int r = 0; r < 4; ++r) xv[r] = ((const u32x4*)xq)[(size_t)(r0 + r) * 56 + lc];
    SB();
    float mean[4], var[4];
#pragma unroll
    for (int r = 0; r < 4; ++r) {
      float s = 0.f;
#pragma unroll
      for (int j = 0; j < 4; ++j) s += bflo(xv[r][j]) + bfhi(xv[r][j]);
      if (lane >= 56) s = 0.f;
      mean[r] = wave_sum(s) * (1.0f / D_);
    }
#pragma unroll
    for (int r = 0; r < 4; ++r) {
      float q = 0.f;
#pragma unroll
      for (int j = 0; j < 4; ++j) {
        const float a = bflo(xv[r][j]) - mean[r], bb = bfhi(xv[r][j]) - mean[r];
        q = __builtin_fmaf(a, a, q);
        q = __builtin_fmaf(bb, bb, q);
      }
      if (lane >= 56) q = 0.f;
      var[r] = wave_sum(q) * (1.0f / D_);
    }
    const float mm = lane == 0 ? mean[0] : lane == 1 ? mean[1] : lane == 2 ? mean[2] : mean[3];
    const float vv = lane == 0 ? var[0] : lane == 1 ? var[1] : lane == 2 ? var[2] : var[3];
    const float rs = ln_rsd(vv);
    if (lane < 4) ((f32x2*)(ws + WS_STAT))[r0 + lane] = f32x2{rb1(mm), rs};
    STAMP(0, 3);
  }
}

constexpr int CV_CS = 4160;
constexpr int CV_UOFF = 8 * CV_CS;
constexpr int CV_CH = CV_UOFF + 286 * 16 + 32;
constexpr int CV_RED = 2 * CV_CH;
constexpr int CV_PRED = CV_RED + 32768;
constexpr int K2_LDS = CV_PRED + 64 * 33 * 4;

NOPK __global__ void __launch_bounds__(512, 2) k2_conv(const u32x4* __restrict__ ssmk, const unsigned short* __restrict__ ssmD,
                                                       const u32x2* __restrict__ wgate, const u32x2* __restrict__ wup,
                                                       const u32* __restrict__ wdown, unsigned char* __restrict__ ws) {
  extern __shared__ __attribute__((aligned(16))) unsigned char smem[];
  const int b = blockIdx.x;
  if (b >= 224) {
    const int tid = threadIdx.x;
    const int pb = b - 224;
    u32x4* dst = (u32x4*)(ws + WS_WD);
    u32 wv[4][8];
#pragma unroll
    for (int it = 0; it < 4; ++it) {
      const int li = it * 512 + tid;
      const int item = pb * 1568 + (li < 1568 ? li : 0);
      const int fo = item / 224, np = item - fo * 224;
#pragma unroll
      for (int j = 0; j < 8; ++j) wv[it][j] = wdown[(size_t)(fo * 8 + j) * 224 + np];
    }
    SB();
#pragma unroll
    for (int it = 0; it < 4; ++it) {
      const int li = it * 512 + tid;
      if (li < 1568) {
        const int item = pb * 1568 + li;
        const int fo = item / 224, np = item - fo * 224;
        u32x4 o0, o1;
#pragma unroll
        for (int j = 0; j < 4; ++j) {
          o0[j] = __builtin_amdgcn_perm(wv[it][2 * j + 1], wv[it][2 * j], 0x05040100u);
          o1[j] = __builtin_amdgcn_perm(wv[it][2 * j + 1], wv[it][2 * j], 0x07060302u);
        }
        const int nn = 2 * np;
        u32x4* p = dst + (((size_t)(nn >> 6) * 112 + (fo >> 1)) * 2 + ((nn >> 5) & 1)) * 64 + (fo & 1) * 32 + (nn & 31);
        p[0] = o0;
        p[1] = o1;
      }
    }
    STAMP(1, 7);
    return;
  }
  const float* __restrict__ mod = (const float*)(ws + WS_MOD);
  const int w = __builtin_amdgcn_readfirstlane(threadIdx.x >> 6);
  const int unit = (b & 7) * 28 + (b >> 3);
  const int d0 = 2 * unit;
  const int lane = threadIdx.x & 63;
  const int n = lane & 15, g = lane >> 4;
  const u32x4* __restrict__ ux = (const u32x4*)(ws + WS_UX) + (size_t)unit * 1024;
  const int t0 = 256 * w + 16 * n + 4 * g;
  u32x2 xe[2];
  f32x4 st[2];
  f32x4* red = (f32x4*)(smem + CV_RED);
  STAMP(1, 0);
  f32x2 SA = *(const f32x2*)(mod + d0);
  f32x2 CAr = *(const f32x2*)(mod + D_ + d0);
  f32x2 GA = *(const f32x2*)(mod + 2 * D_ + d0);
  u32 DDr = *(const u32*)(ssmD + d0);
  SB();
  if (w >= 4) {
    const int tid = threadIdx.x - 256;
    const int mat = unit / 112, ft = unit % 112;
    const u32x2* __restrict__ W = mat ? wup : wgate;
    float* pred = (float*)(smem + CV_PRED);
    const int fq = tid & 3, ko = tid >> 2;
    const int kc = ko < 56 ? ko : 55;
    u32x2 wv[8];
    const f32x4 cbA = *(const f32x4*)(mod + 4 * D_ + kc * 8), cbB = *(const f32x4*)(mod + 4 * D_ + kc * 8 + 4);
    const f32x4 sbA = *(const f32x4*)(mod + 3 * D_ + kc * 8), sbB = *(const f32x4*)(mod + 3 * D_ + kc * 8 + 4);
#pragma unroll
    for (int j = 0; j < 8; ++j) wv[j] = W[(size_t)(kc * 8 + j) * 448 + ft * 4 + fq];
    SB();
#pragma unroll
    for (int cc = 0; cc < 2; ++cc) xe[cc] = ((const u32x2*)(ux + (t0 >> 5) * 16 + cc * 8 + 4 + ((t0 >> 3) & 3)))[g & 1];
    st[0] = ((const f32x4*)(ws + WS_STAT))[t0 / 2];
    st[1] = ((const f32x4*)(ws + WS_STAT))[t0 / 2 + 1];
    SB();
    float sc[8], sb[8];
#pragma unroll
    for (int j = 0; j < 4; ++j) {
      sc[j] = rb1(1.0f + cbA[j]);
      sc[j + 4] = rb1(1.0f + cbB[j]);
      sb[j] = sbA[j];
      sb[j + 4] = sbB[j];
    }
    float c1[4] = {0.f, 0.f, 0.f, 0.f}, c2[4] = {0.f, 0.f, 0.f, 0.f};
    u32x4 o[4];
#pragma unroll
    for (int jj = 0; jj < 4; ++jj) {
      const float a[4] = {bflo(wv[2 * jj].x), bfhi(wv[2 * jj].x), bflo(wv[2 * jj].y), bfhi(wv[2 * jj].y)};
      const float bb[4] = {bflo(wv[2 * jj + 1].x), bfhi(wv[2 * jj + 1].x), bflo(wv[2 * jj + 1].y), bfhi(wv[2 * jj + 1].y)};
#pragma unroll
      for (int f = 0; f < 4; ++f) {
        const u32 pp = pk2(a[f] * sc[2 * jj], bb[f] * sc[2 * jj + 1]);
        o[f][jj] = pp;
        c1[f] = dot2(pp, 0x3f803f80u, c1[f]);
        c2[f] = __builtin_fmaf(a[f], sb[2 * jj], c2[f]);
        c2[f] = __builtin_fmaf(bb[f], sb[2 * jj + 1], c2[f]);
      }
    }
    if (ko < 56) {
      u32x4* dst = (u32x4*)(ws + WS_WGU) + (((size_t)ft * 14 + (ko >> 2)) * 2 + mat) * 64 + (ko & 3) * 16 + fq * 4;
#pragma unroll
      for (int f = 0; f < 4; ++f) dst[f] = o[f];
    }
#pragma unroll
    for (int f = 0; f < 4; ++f) {
      pred[ko * 33 + fq * 4 + f] = ko < 56 ? c1[f] : 0.f;
      pred[ko * 33 + 16 + fq * 4 + f] = ko < 56 ? c2[f] : 0.f;
    }
    STAMP(1, 1);
    bar();
    const int col = tid >> 3, part = tid & 7;
    float s = 0.f;
#pragma unroll
    for (int i = 0; i < 8; ++i) s += pred[(part * 8 + i) * 33 + col];
    s += dppf<0xB1>(s);
    s += dppf<0x4E>(s);
    s += dppf<0x141>(s);
    if (part == 0) ((float*)(ws + WS_C12))[(mat * 2 + (col >> 4)) * F_ + ft * 16 + (col & 15)] = s;
  } else {
    const int ch = w >> 1;
    const int i = threadIdx.x & 127;
    unsigned char* cb = smem + ch * CV_CH;
    const u32x4* kr = ssmk + (size_t)(d0 + ch) * 256;
    u32x4 ka[2], kb[2], uu[2];
    ka[0] = kr[i > 0 ? i - 1 : 0];
    kb[0] = kr[i];
    ka[1] = kr[i + 127];
    kb[1] = kr[i + 128];
    uu[0] = ux[(i >> 2) * 16 + ch * 8 + (i & 3)];
    uu[1] = ux[(32 + (i >> 2)) * 16 + ch * 8 + (i & 3)];
    SB();
#pragma unroll
    for (int cc = 0; cc < 2; ++cc) xe[cc] = ((const u32x2*)(ux + (t0 >> 5) * 16 + cc * 8 + 4 + ((t0 >> 3) & 3)))[g & 1];
    st[0] = ((const f32x4*)(ws + WS_STAT))[t0 / 2];
    st[1] = ((const f32x4*)(ws + WS_STAT))[t0 / 2 + 1];
    SB();
    if (i < 38) {
      unsigned char* zp = i < 8 ? cb + i * CV_CS : cb + CV_UOFF + (256 + i - 8) * 16;
      *(u32x4*)zp = u32x4{0u, 0u, 0u, 0u};
    }
    if (i == 0) ka[0] = u32x4{0u, 0u, 0u, 0u};
#pragma unroll
    for (int it = 0; it < 2; ++it) {
      const int qp = i + 1 + 128 * it;
      const u32 Dw[8] = {ka[it].x, ka[it].y, ka[it].z, ka[it].w, kb[it].x, kb[it].y, kb[it].z, kb[it].w};
      u32 E[7];
#pragma unroll
      for (int j = 0; j < 7; ++j) E[j] = __builtin_amdgcn_alignbit(Dw[j + 1], Dw[j], 16);
      unsigned char* p = cb + qp * 16;
#pragma unroll
      for (int s2 = 0; s2 < 4; ++s2) {
        *(u32x4*)(p + (2 * s2) * CV_CS) = u32x4{E[s2], E[s2 + 1], E[s2 + 2], E[s2 + 3]};
        *(u32x4*)(p + (2 * s2 + 1) * CV_CS) = u32x4{Dw[s2 + 1], Dw[s2 + 2], Dw[s2 + 3], Dw[s2 + 4]};
      }
    }
#pragma unroll
    for (int it = 0; it < 2; ++it) *(u32x4*)(cb + CV_UOFF + (255 - (i + 128 * it)) * 16) = uu[it];
    STAMP(1, 2);
    bar();
    STAMP(1, 3);

    const int sh = w & 1;
    const unsigned char* ap = cb + (n & 7) * CV_CS + ((n >> 3) + g) * 16 + sh * 256;
    const unsigned char* bp = cb + CV_UOFF + (254 - 2 * n + g) * 16 - 3584 + sh * 256;
    f32x4 acc[8];
#pragma unroll
    for (int c = 0; c < 8; ++c) acc[c] = f32x4{0.f, 0.f, 0.f, 0.f};
    u32x4 A[2][8], B[2][8];
#pragma unroll
    for (int e = 0; e < 8; ++e) {
      A[0][e] = *(const u32x4*)(ap + 512 * e);
      B[0][e] = *(const u32x4*)(bp + 512 * (7 - e));
      SB();
    }
#pragma unroll
    for (int s = 0; s < 4; ++s) {
      const int cu = s & 1, nx = cu ^ 1;
      int cnt = 0;
#pragma unroll
      for (int c = 0; c < 8; ++c) {
#pragma unroll
        for (int ii = 0; ii <= c; ++ii) {
          acc[c] = mfma16(A[cu][ii], B[cu][c - ii], acc[c]);
          if (s < 3 && cnt >= 2 && cnt < 34 && (cnt & 1) == 0) {
            const int e = (cnt - 2) >> 2;
            if (((cnt - 2) >> 1) & 1) B[nx][e] = *(const u32x4*)(bp + 64 * (s + 1) + 512 * (7 - e));
            else A[nx][e] = *(const u32x4*)(ap + 64 * (s + 1) + 512 * e);
            SB();
          }
          ++cnt;
        }
        if (s == 3) { red[(w * 8 + c) * 64 + lane] = acc[c]; SB(); }
      }
      SB();
    }
    STAMP(1, 4);
  }
  bar();
  STAMP(1, 5);
  {
    const f32x2 CA = rb2(CAr + 1.0f);
    const f32x2 DD = up2(DDr);
    u32* x1f = (u32*)(ws + WS_X1F);
    const int ks = d0 >> 5, gk = (d0 >> 3) & 3, jj = (d0 & 7) >> 1;
    const f32x4 y0 = red[(0 * 8 + w) * 64 + lane] + red[(1 * 8 + w) * 64 + lane];
    const f32x4 y1 = red[(2 * 8 + w) * 64 + lane] + red[(3 * 8 + w) * 64 + lane];
    const int uo = CV_UOFF + (255 - (t0 >> 3)) * 16 + (1 - (g & 1)) * 8;
    const u32x2 ur0 = *(const u32x2*)(smem + uo);
    const u32x2 ur1 = *(const u32x2*)(smem + CV_CH + uo);
    const float u0[4] = {bfhi(ur0.y), bflo(ur0.y), bfhi(ur0.x), bflo(ur0.x)};
    const float u1[4] = {bfhi(ur1.y), bflo(ur1.y), bfhi(ur1.x), bflo(ur1.x)};
    const float xa[4] = {bflo(xe[0].x), bfhi(xe[0].x), bflo(xe[0].y), bfhi(xe[0].y)};
    const float xb[4] = {bflo(xe[1].x), bfhi(xe[1].x), bflo(xe[1].y), bfhi(xe[1].y)};
    const float mn[4] = {st[0].x, st[0].z, st[1].x, st[1].z};
    const float rs[4] = {st[0].y, st[0].w, st[1].y, st[1].w};
    u32* dst = x1f + (((size_t)(16 * w + 4 * g) * 14 + ks) * 64 + gk * 16 + n) * 4 + jj;
#pragma unroll
    for (int r = 0; r < 4; ++r) {
      const float h0 = __builtin_fmaf(xa[r] - mn[r], rs[r] * CA.x, SA.x);
      const float h1 = __builtin_fmaf(xb[r] - mn[r], rs[r] * CA.y, SA.y);
      const float z0 = __builtin_fmaf(u0[r], DD.x, y0[r]);
      const float z1 = __builtin_fmaf(u1[r], DD.y, y1[r]);
      dst[(size_t)r * 14 * 256] = pk2(__builtin_fmaf(h0 * GA.x, z0, xa[r]), __builtin_fmaf(h1 * GA.y, z1, xb[r]));
    }
  }
  STAMP(1, 6);
}

constexpr int K3_XP = 4 * 14 * 1024;
constexpr int K3_ST = 131072;
constexpr int K3_LDS = K3_ST + 2048;
constexpr int K3_PF = 6;
constexpr int AUX_SC1 = 16;
constexpr unsigned SPIN_CAP = 1u << 18;

typedef __attribute__((address_space(1))) u32 gu32;
DEV u32x4 bld(__amdgpu_buffer_rsrc_t rs, int voff, int soff) { return __builtin_amdgcn_raw_buffer_load_b128(rs, voff, soff, 0); }
DEV u32x4 bld_sc1(__amdgpu_buffer_rsrc_t rs, int voff, int soff) { return __builtin_amdgcn_raw_buffer_load_b128(rs, voff, soff, AUX_SC1); }
DEV u32 flag_ld(const unsigned char* ws, int idx) {
  return __hip_atomic_load((gu32*)(ws + WS_FLG) + idx, __ATOMIC_RELAXED, __HIP_MEMORY_SCOPE_AGENT);
}
DEV void flag_wait(const unsigned char* ws, int idx, u32 v) {
  for (unsigned spins = 0; spins < SPIN_CAP; ++spins) {
    if (__builtin_amdgcn_ballot_w64(v == 0u) == 0ull) break;
    __builtin_amdgcn_s_sleep(2);
    v = flag_ld(ws, idx);
  }
  __builtin_amdgcn_fence(__ATOMIC_ACQUIRE, "wavefront");
}

NOPK __global__ void __launch_bounds__(512, 2) k34_ffn(unsigned char* __restrict__ ws, u32x2* __restrict__ out) {
  extern __shared__ __attribute__((aligned(16))) unsigned char smem[];
  const int tid = threadIdx.x;
  const int b = blockIdx.x;
  const int xcd = b & 7, idx = b >> 3;
  STAMP(2, 0);
  const int grp = 4 * (xcd & 3) + idx / 7, mh = xcd >> 2, nt64 = idx % 7, Fs = 7 * mh + nt64;
  const int lane = tid & 63;
  const int w = __builtin_amdgcn_readfirstlane(tid >> 6);
  const int ft = Fs * 8 + w;
  const __amdgpu_buffer_rsrc_t rs = __builtin_amdgcn_make_buffer_rsrc((void*)ws, 0, (int)WS_END, 0x00020000);
  const int vo = lane * 16;
  const int xoff = (int)WS_X1F + ((grp * 4 + (w >> 1)) * 14 + 7 * (w & 1)) * 1024;
  const int woff = (int)WS_WGU + ft * 28 * 1024;
  const int doff = (int)WS_WD + (nt64 * 112 + 14 * w) * 2048;
  const int aoff = (int)WS_ACT + ((grp * 2 + mh) * 112 + 14 * w) * 1024;
  u32x4* xl = (u32x4*)smem;
  f32x2* stl = (f32x2*)(smem + K3_ST);
  const int g = lane >> 4, m16 = lane & 15;

  u32x4 xs[7], wf[28];
#pragma unroll
  for (int k = 0; k < 7; ++k) { xs[k] = bld(rs, vo, xoff + k * 1024); SB(); }
  __builtin_amdgcn_s_barrier();
#pragma unroll
  for (int k = 0; k < 2 * K3_PF; ++k) { wf[k] = bld(rs, vo, woff + k * 1024); SB(); }
  const float* c12 = (const float*)(ws + WS_C12) + ft * 16 + 4 * g;
  const f32x4 c1g = *(const f32x4*)(c12), c2g = *(const f32x4*)(c12 + F_);
  const f32x4 c1u = *(const f32x4*)(c12 + 2 * F_), c2u = *(const f32x4*)(c12 + 3 * F_);
  SB();
  const int actv = ((g >> 1) * 32 + m16) * 16 + (g & 1) * 8;
  const int acts = (int)WS_ACT + ((grp * 2) * 112 + ft) * 1024;
  const int fidx = 14 * w + (lane < 14 ? lane : 13);

#define K3_STAGE(ps)                                                                       \
  {                                                                                        \
    f32x4 sm = {0.f, 0.f, 0.f, 0.f}, sq = {0.f, 0.f, 0.f, 0.f};                            \
    const u32x4 ones = {0x3f803f80u, 0x3f803f80u, 0x3f803f80u, 0x3f803f80u};               \
    _Pragma("unroll") for (int k = 0; k < 7; ++k) {                                        \
      xl[(((ps) * 14 + 7 * (w & 1) + k) * 4 + (w >> 1)) * 64 + lane] = xs[k];              \
      sm = mfma16(ones, xs[k], sm);                                                        \
      sq = mfma16(xs[k], xs[k], sq);                                                       \
    }                                                                                      \
    const float dg = (m16 & 2) ? ((m16 & 1) ? sq[3] : sq[2]) : ((m16 & 1) ? sq[1] : sq[0]); \
    if (g == (m16 >> 2)) stl[((ps) * 2 + (w & 1)) * 64 + (w >> 1) * 16 + m16] = f32x2{sm[0], dg}; \
  }
  K3_STAGE(0);
  STAMP(2, 1);
  bar();
  STAMP(2, 2);

  u32 fl = 0u;
  u32x4 af[14];
#pragma unroll
  for (int ps = 0; ps < 2; ++ps) {
    f32x4 ag[4], au[4];
#pragma unroll
    for (int m = 0; m < 4; ++m) {
      ag[m] = f32x4{0.f, 0.f, 0.f, 0.f};
      au[m] = f32x4{0.f, 0.f, 0.f, 0.f};
    }
    const u32x4* xp = xl + ps * 14 * 4 * 64 + lane;
    u32x4 xf[2][4];
#pragma unroll
    for (int m = 0; m < 4; ++m) xf[0][m] = xp[m * 64];
    const f32x2 pa = stl[(ps * 2) * 64 + lane], pb = stl[(ps * 2 + 1) * 64 + lane];
    SB();
    float sdv[4], nmv[4];
#pragma unroll
    for (int k = 0; k < 14; ++k) {
      const int cu = k & 1, nx = cu ^ 1;
      if (ps == 0) {
        if (k + K3_PF < 14) {
          wf[2 * (k + K3_PF)] = bld(rs, vo, woff + 2 * (k + K3_PF) * 1024);
          wf[2 * (k + K3_PF) + 1] = bld(rs, vo, woff + (2 * (k + K3_PF) + 1) * 1024);
        }
        if (k < 7) xs[k] = bld(rs, vo, xoff + (64 * 14 + k) * 1024);
        SB();
        if (k == 11) { K3_STAGE(1); SB(); }
        if (k == 12) { bar(); SB(); }
      } else {
        if (k == 3) {
          asm volatile("s_waitcnt vmcnt(0)" ::: "memory");
          if (lane == 0) __hip_atomic_store((gu32*)(ws + WS_FLG) + (grp * 2 + 0) * 128 + ft, 1u, __ATOMIC_RELAXED, __HIP_MEMORY_SCOPE_AGENT);
#pragma unroll
          for (int q = 0; q < 4; ++q) wf[q] = bld(rs, vo, doff + q * 1024);
          SB();
        }
        if (k >= 3) {
          wf[2 * (k - 1)] = bld(rs, vo, doff + 2 * (k - 1) * 1024);
          wf[2 * (k - 1) + 1] = bld(rs, vo, doff + (2 * (k - 1) + 1) * 1024);
          SB();
        }
        if (k == 11) { fl = flag_ld(ws, (grp * 2 + 0) * 128 + fidx); SB(); }
      }
      if (k == 1) {
        const float mean = (pa.x + pb.x) * (1.0f / D_);
        const float var = __builtin_fmaxf(__builtin_fmaf(-mean, mean, (pa.y + pb.y) * (1.0f / D_)), 0.f);
        rows_bcast(ln_sd(var), sdv);
        rows_bcast(-rb1(mean), nmv);
      }
#pragma unroll
      for (int m = 0; m < 4; ++m) {
        if (k < 13) xf[nx][m] = xp[((k + 1) * 4 + m) * 64];
        if (k == 4 + m) {
#pragma unroll
          for (int r = 0; r < 4; ++r) {
            ag[m][r] = __builtin_fmaf(c2g[r], sdv[m], __builtin_fmaf(c1g[r], nmv[m], ag[m][r]));
            au[m][r] = __builtin_fmaf(c2u[r], sdv[m], __builtin_fmaf(c1u[r], nmv[m], au[m][r]));
          }
        }
        ag[m] = mfma16(wf[2 * k], xf[cu][m], ag[m]);
        au[m] = mfma16(wf[2 * k + 1], xf[cu][m], au[m]);
        if (k != 1) SB();
      }
      SB();
    }
    STAMP(2, 3 + 2 * ps);
    if (ps == 1) {
      wf[26] = bld(rs, vo, doff + 26 * 1024);
      wf[27] = bld(rs, vo, doff + 27 * 1024);
      flag_wait(ws, (grp * 2 + 0) * 128 + fidx, fl);
#pragma unroll
      for (int q = 0; q < 14; ++q) { af[q] = bld_sc1(rs, vo, aoff + q * 1024); SB(); }
    }
#pragma unroll
    for (int m = 0; m < 4; ++m) {
      const float rsd = __builtin_amdgcn_rcpf(sdv[m]);
      const float K0 = (-2.0f * 0.7978845608028654f * 1.4426950408889634f) * rsd;
      const float K1 = K0 * 0.044715f * rsd * rsd;
      const float S2 = sdv[m] * sdv[m];
      float o[4];
#pragma unroll
      for (int r = 0; r < 4; ++r) {
        const float a = ag[m][r];
        const float e = __builtin_amdgcn_exp2f(a * __builtin_fmaf(a * a, K1, K0));
        o[r] = (a * au[m][r]) * __builtin_amdgcn_rcpf(__builtin_fmaf(e, S2, S2));
      }
      __builtin_amdgcn_raw_buffer_store_b64(u32x2{pk2(o[0], o[1]), pk2(o[2], o[3])}, rs, actv,
                                            acts + (ps * 32 + (m >> 1)) * 112 * 1024 + (m & 1) * 256, AUX_SC1);
    }
    SB();
    STAMP(2, 4 + 2 * ps);
  }
  asm volatile("s_waitcnt vmcnt(0)" ::: "memory");
  if (lane == 0) __hip_atomic_store((gu32*)(ws + WS_FLG) + (grp * 2 + 1) * 128 + ft, 1u, __ATOMIC_RELAXED, __HIP_MEMORY_SCOPE_AGENT);
  const int mr = tid >> 4, c4 = tid & 15;
  const int nn = nt64 * 64 + 4 * c4;
  u32x2 xr[2];
#pragma unroll
  for (int ps = 0; ps < 2; ++ps) {
    const int mu = ps * 1024 + grp * 64 + mh * 32 + mr;
    xr[ps] = ((const u32x2*)(ws + WS_X1F))[((((size_t)(mu >> 4) * 14 + (nn >> 5)) * 64 + ((nn >> 3) & 3) * 16 + (mu & 15)) << 1) + (c4 & 1)];
  }
  const f32x4 gb = *(const f32x4*)((const float*)(ws + WS_MOD) + 5 * D_ + nn);
  SB();
  STAMP(3, 0);
  bar();
  STAMP(3, 1);
  const int m32 = lane & 31, h = lane >> 5;

#pragma unroll
  for (int ps = 0; ps < 2; ++ps) {
    f32x16 acc[2];
#pragma unroll
    for (int c = 0; c < 2; ++c)
#pragma unroll
      for (int r = 0; r < 16; ++r) acc[c][r] = 0.f;
    if (ps == 0) { fl = flag_ld(ws, (grp * 2 + 1) * 128 + fidx); SB(); }
#pragma unroll
    for (int k = 0; k < 14; ++k) {
      acc[0] = mfma32(wf[2 * k], af[k], acc[0]);
      acc[1] = mfma32(wf[2 * k + 1], af[k], acc[1]);
      SB();
    }
    STAMP(3, 2 + 3 * ps);
    if (ps == 0) {
      flag_wait(ws, (grp * 2 + 1) * 128 + fidx, fl);
#pragma unroll
      for (int q = 0; q < 14; ++q) { af[q] = bld_sc1(rs, vo, aoff + (32 * 112 + q) * 1024); SB(); }
      STAMP(3, 3);
    }
    f32x4* red = (f32x4*)(smem + ps * 65536);
#pragma unroll
    for (int nt = 0; nt < 2; ++nt)
#pragma unroll
      for (int q = 0; q < 4; ++q) {
        const int cc = nt * 8 + q * 2 + h;
        red[w * 512 + cc * 32 + (m32 ^ cc)] = f32x4{acc[nt][4 * q], acc[nt][4 * q + 1], acc[nt][4 * q + 2], acc[nt][4 * q + 3]};
      }
    bar();
    f32x4 pv[8];
#pragma unroll
    for (int ww = 0; ww < 8; ++ww) pv[ww] = red[ww * 512 + c4 * 32 + (mr ^ c4)];
    const f32x4 s = ((pv[0] + pv[1]) + (pv[2] + pv[3])) + ((pv[4] + pv[5]) + (pv[6] + pv[7]));
    const int mu = ps * 1024 + grp * 64 + mh * 32 + mr;
    const int t = (mu & ~255) | ((mu & 15) << 4) | ((mu >> 4) & 15);
    const float xv[4] = {bflo(xr[ps].x), bfhi(xr[ps].x), bflo(xr[ps].y), bfhi(xr[ps].y)};
    float o[4];
#pragma unroll
    for (int r = 0; r < 4; ++r) o[r] = __builtin_fmaf(gb[r], s[r], xv[r]);
    out[((size_t)t * D_ + nn) >> 2] = u32x2{pk2(o[0], o[1]), pk2(o[2], o[3])};
    STAMP(3, 4 + 2 * ps);
  }
}

extern "C" void kernel_launch(void* const* d_in, const int* in_sizes, int n_in, void* d_out, int out_size, void* d_ws,
                              size_t ws_size, hipStream_t stream) {
  (void)in_sizes; (void)n_in; (void)out_size; (void)ws_size;
  const void* A_q = d_in[0];
  const void* A_k = d_in[1];
  const void* A_v = d_in[2];
  const void* A_shift = d_in[3];
  const void* A_ssmk = d_in[4];
  const void* A_ssmD = d_in[5];
  const void* W_gate = d_in[7];
  const void* W_up = d_in[8];
  const void* W_down = d_in[9];
  const void* cvec = d_in[10];
  const void* w_mod = d_in[11];
  unsigned char* ws = (unsigned char*)d_ws;

  (void)hipFuncSetAttribute((const void*)k2_conv, hipFuncAttributeMaxDynamicSharedMemorySize, K2_LDS);
  (void)hipFuncSetAttribute((const void*)k34_ffn, hipFuncAttributeMaxDynamicSharedMemorySize, K3_LDS);

  k1_prep<<<K1_TR + K1_MOD + K1_ST, 256, 0, stream>>>((const u32*)A_q, (const u32*)A_k, (const u32*)A_v,
                                                       (const u32x4*)A_shift, (const unsigned short*)cvec,
                                                       (const u32x4*)w_mod, ws);
  k2_conv<<<256, 512, K2_LDS, stream>>>((const u32x4*)A_ssmk, (const unsigned short*)A_ssmD,
                                        (const u32x2*)W_gate, (const u32x2*)W_up, (const u32*)W_down, ws);
  k34_ffn<<<224, 512, K3_LDS, stream>>>(ws, (u32x2*)d_out);
}
```

```cpp
#include <hip/hip_runtime.h>
#include <hip/hip_bf16.h>

typedef unsigned int u32;
typedef u32 u32x2 __attribute__((ext_vector_type(2)));
typedef u32 u32x4 __attribute__((ext_vector_type(4)));
typedef float f32x2 __attribute__((ext_vector_type(2)));
typedef float f32x4 __attribute__((ext_vector_type(4)));
typedef float f32x16 __attribute__((ext_vector_type(16)));
typedef __bf16 bf16x2 __attribute__((ext_vector_type(2)));
typedef __bf16 bf16x8 __attribute__((ext_vector_type(8)));

#if defined(__HIP_DEVICE_COMPILE__)
#define NOPK __attribute__((target("no-packed-fp32-ops")))
#else
#define NOPK
#endif
#define DEV NOPK __device__ __forceinline__
#define SB() __builtin_amdgcn_sched_barrier(0)

#define STAMP(kid, slot)

constexpr int S_ = 2048, D_ = 448, F_ = 1792;
constexpr float EPS_ = 1e-5f;

constexpr size_t WS_MOD = 0;
constexpr size_t WS_STAT = 16384;
constexpr size_t WS_C12 = 32768;
constexpr size_t WS_FLG = 65536;
constexpr size_t WS_UX = 65536 + 16384;
constexpr size_t WS_X1F = WS_UX + (size_t)224 * 16384;
constexpr size_t WS_WGU = WS_X1F + (size_t)128 * 14 * 1024;
constexpr size_t WS_WD = WS_WGU + (size_t)112 * 2 * 14 * 1024;
constexpr size_t WS_ACT = WS_WD + (size_t)14 * 112 * 1024;
constexpr size_t WS_END = WS_ACT + (size_t)64 * 112 * 1024;

DEV float bflo(u32 v) { return __builtin_bit_cast(float, v << 16); }
DEV float bfhi(u32 v) { return __builtin_bit_cast(float, v & 0xffff0000u); }
DEV u32 pk2(float a, float b) {
  f32x2 v = {a, b};
  bf16x2 r = __builtin_convertvector(v, bf16x2);
  return __builtin_bit_cast(u32, r);
}
DEV f32x2 up2(u32 v) { return f32x2{bflo(v), bfhi(v)}; }
DEV f32x2 rb2(f32x2 v) { return up2(pk2(v.x, v.y)); }
DEV float rb1(float a) { return bflo(pk2(a, a)); }
DEV float bf2f(unsigned short b) { return __builtin_bit_cast(float, (u32)b << 16); }

DEV f32x4 mfma16(u32x4 a, u32x4 b, f32x4 c) {
  return __builtin_amdgcn_mfma_f32_16x16x32_bf16(__builtin_bit_cast(bf16x8, a), __builtin_bit_cast(bf16x8, b), c, 0, 0, 0);
}
DEV f32x16 mfma32(u32x4 a, u32x4 b, f32x16 c) {
  return __builtin_amdgcn_mfma_f32_32x32x16_bf16(__builtin_bit_cast(bf16x8, a), __builtin_bit_cast(bf16x8, b), c, 0, 0, 0);
}
DEV float dot2(u32 a, u32 b, float c) {
  return __builtin_amdgcn_fdot2_f32_bf16(__builtin_bit_cast(bf16x2, a), __builtin_bit_cast(bf16x2, b), c, false);
}
template <int CTRL> DEV float dppf(float v) {
  return __builtin_bit_cast(float, __builtin_amdgcn_update_dpp(0, __builtin_bit_cast(int, v), CTRL, 0xf, 0xf, true));
}
DEV float row16_sum(float v) {
  v += dppf<0xB1>(v);
  v += dppf<0x4E>(v);
  v += dppf<0x141>(v);
  v += dppf<0x140>(v);
  return v;
}
DEV float rdlane(float v, int l) {
  return __builtin_bit_cast(float, __builtin_amdgcn_readlane(__builtin_bit_cast(int, v), l));
}
DEV float wave_sum(float v) {
  v = row16_sum(v);
  return (rdlane(v, 0) + rdlane(v, 16)) + (rdlane(v, 32) + rdlane(v, 48));
}
DEV float col4_sum(float v) {
  u32 a = __builtin_bit_cast(u32, v);
  auto r = __builtin_amdgcn_permlane16_swap(a, a, false, false);
  v = __builtin_bit_cast(float, (u32)r[0]) + __builtin_bit_cast(float, (u32)r[1]);
  a = __builtin_bit_cast(u32, v);
  auto q = __builtin_amdgcn_permlane32_swap(a, a, false, false);
  return __builtin_bit_cast(float, (u32)q[0]) + __builtin_bit_cast(float, (u32)q[1]);
}
DEV void bar() {
  asm volatile("s_waitcnt lgkmcnt(0)" ::: "memory");
  __builtin_amdgcn_s_barrier();
  asm volatile("" ::: "memory");
}
DEV float ln_sd(float var) { return rb1(__builtin_amdgcn_sqrtf(rb1(rb1(var) + EPS_))); }
DEV float ln_rsd(float var) { return __builtin_amdgcn_rcpf(ln_sd(var)); }
DEV void rows_bcast(float v, float (&o)[4]) {
  const u32 a = __builtin_bit_cast(u32, v);
  auto r = __builtin_amdgcn_permlane16_swap(a, a, false, false);
  auto e = __builtin_amdgcn_permlane32_swap((u32)r[0], (u32)r[0], false, false);
  auto d = __builtin_amdgcn_permlane32_swap((u32)r[1], (u32)r[1], false, false);
  o[0] = __builtin_bit_cast(float, (u32)e[0]);
  o[2] = __builtin_bit_cast(float, (u32)e[1]);
  o[1] = __builtin_bit_cast(float, (u32)d[0]);
  o[3] = __builtin_bit_cast(float, (u32)d[1]);
}

constexpr int K1_TR = 256;
constexpr int K1_ST = 128;
constexpr int K1_MOD = 84;

NOPK __global__ void __launch_bounds__(256) k1_prep(const u32* __restrict__ xq, const u32* __restrict__ kin,
                                                    const u32* __restrict__ vin, const u32x4* __restrict__ shiftk,
                                                    const unsigned short* __restrict__ cvec,
                                                    const u32x4* __restrict__ wmod, unsigned char* __restrict__ ws) {
  __shared__ float red[64 * 33];
  const int tid = threadIdx.x;
  const int b = blockIdx.x;
  STAMP(0, 0);
  if (b >= K1_MOD && b < K1_MOD + K1_TR) {
    if (tid >= 224) return;
    const int rg = (b - K1_MOD) >> 2, cq = (b - K1_MOD) & 3;
    const int p = cq * 56 + (tid >> 2), j = tid & 3;
    const int t0 = rg * 32 + j * 8;
    u32 kk[11], vv[8], xx[8];
#pragma unroll
    for (int r = 0; r < 11; ++r) {
      const int t = t0 - 3 + r;
      kk[r] = kin[(size_t)(t < 0 ? 0 : t) * 224 + p];
    }
#pragma unroll
    for (int r = 0; r < 8; ++r) vv[r] = vin[(size_t)(t0 + r) * 224 + p];
    const u32x4 sw = shiftk[p];
#pragma unroll
    for (int r = 0; r < 8; ++r) xx[r] = xq[(size_t)(t0 + r) * 224 + p];
    SB();
    if (t0 == 0) { kk[0] = 0u; kk[1] = 0u; kk[2] = 0u; }
    const f32x2 w0 = {bflo(sw.x), bflo(sw.z)}, w1 = {bfhi(sw.x), bfhi(sw.z)};
    const f32x2 w2 = {bflo(sw.y), bflo(sw.w)}, w3 = {bfhi(sw.y), bfhi(sw.w)};
    f32x2 kf[11];
#pragma unroll
    for (int r = 0; r < 11; ++r) kf[r] = up2(kk[r]);
    u32 kv[8];
#pragma unroll
    for (int r = 0; r < 8; ++r) {
      float a0 = kf[r].x * w3.x, a1 = kf[r].y * w3.y;
      a0 = __builtin_fmaf(kf[r + 1].x, w2.x, a0); a1 = __builtin_fmaf(kf[r + 1].y, w2.y, a1);
      a0 = __builtin_fmaf(kf[r + 2].x, w1.x, a0); a1 = __builtin_fmaf(kf[r + 2].y, w1.y, a1);
      a0 = __builtin_fmaf(kf[r + 3].x, w0.x, a0); a1 = __builtin_fmaf(kf[r + 3].y, w0.y, a1);
      kv[r] = pk2(a0 * bflo(vv[r]), a1 * bfhi(vv[r]));
    }
    u32x4 u0, u1, x0, x1;
#pragma unroll
    for (int i = 0; i < 4; ++i) {
      u0[i] = __builtin_amdgcn_perm(kv[6 - 2 * i], kv[7 - 2 * i], 0x05040100u);
      u1[i] = __builtin_amdgcn_perm(kv[6 - 2 * i], kv[7 - 2 * i], 0x07060302u);
      x0[i] = __builtin_amdgcn_perm(xx[2 * i + 1], xx[2 * i], 0x05040100u);
      x1[i] = __builtin_amdgcn_perm(xx[2 * i + 1], xx[2 * i], 0x07060302u);
    }
    u32x4* dst = (u32x4*)(ws + WS_UX) + ((size_t)p * 64 + rg) * 16 + j;
    dst[0] = u0;
    dst[4] = x0;
    dst[8] = u1;
    dst[12] = x1;
    STAMP(0, 2);
    return;
  }
  if (b < K1_MOD) {
    const int mb = b;
    const int cq = tid & 3, rg = tid >> 2;
    u32x4 w[7];
    unsigned short cs[7];
#pragma unroll
    for (int i = 0; i < 7; ++i) cs[i] = cvec[rg + 64 * i];
    SB();
#pragma unroll
    for (int i = 0; i < 7; ++i) w[i] = wmod[(size_t)(rg + 64 * i) * 336 + mb * 4 + cq];
    SB();
    float scv[7];
#pragma unroll
    for (int i = 0; i < 7; ++i) {
      const float c = bf2f(cs[i]);
      scv[i] = rb1(c * __builtin_amdgcn_rcpf(1.0f + __builtin_amdgcn_exp2f(-1.4426950408889634f * c)));
    }
    SB();
    float acc[8];
#pragma unroll
    for (int j = 0; j < 8; ++j) acc[j] = 0.f;
#pragma unroll
    for (int i = 0; i < 7; ++i) {
      const float sc = scv[i];
#pragma unroll
      for (int j = 0; j < 4; ++j) {
        acc[2 * j] = __builtin_fmaf(sc, bflo(w[i][j]), acc[2 * j]);
        acc[2 * j + 1] = __builtin_fmaf(sc, bfhi(w[i][j]), acc[2 * j + 1]);
      }
    }
#pragma unroll
    for (int j = 0; j < 8; ++j) red[rg * 33 + cq * 8 + j] = acc[j];
    bar();
    const int col = tid >> 3, part = tid & 7;
    float s = 0.f;
#pragma unroll
    for (int i = 0; i < 8; ++i) s += red[(part * 8 + i) * 33 + col];
    s += dppf<0xB1>(s);
    s += dppf<0x4E>(s);
    s += dppf<0x141>(s);
    if (part == 0) ((float*)(ws + WS_MOD))[mb * 32 + col] = rb1(s);
    STAMP(0, 1);
    return;
  }
  {
    const int sb = b - K1_MOD - K1_TR;
    const int lane = tid & 63, w = tid >> 6;
    const int r0 = sb * 16 + w * 4;
    const int lc = lane < 56 ? lane : 55;
    if (tid < 32) ((u32*)(ws + WS_FLG))[sb * 32 + tid] = 0u;
    u32x4 xv[4];
#pragma unroll
    for (int r = 0; r < 4; ++r) xv[r] = ((const u32x4*)xq)[(size_t)(r0 + r) * 56 + lc];
    SB();
    float mean[4], var[4];
#pragma unroll
    for (int r = 0; r < 4; ++r) {
      float s = 0.f;
#pragma unroll
      for (int j = 0; j < 4; ++j) s += bflo(xv[r][j]) + bfhi(xv[r][j]);
      if (lane >= 56) s = 0.f;
      mean[r] = wave_sum(s) * (1.0f / D_);
    }
#pragma unroll
    for (int r = 0; r < 4; ++r) {
      float q = 0.f;
#pragma unroll
      for (int j = 0; j < 4; ++j) {
        const float a = bflo(xv[r][j]) - mean[r], bb = bfhi(xv[r][j]) - mean[r];
        q = __builtin_fmaf(a, a, q);
        q = __builtin_fmaf(bb, bb, q);
      }
      if (lane >= 56) q = 0.f;
      var[r] = wave_sum(q) * (1.0f / D_);
    }
    const float mm = lane == 0 ? mean[0] : lane == 1 ? mean[1] : lane == 2 ? mean[2] : mean[3];
    const float vv = lane == 0 ? var[0] : lane == 1 ? var[1] : lane == 2 ? var[2] : var[3];
    const float rs = ln_rsd(vv);
    if (lane < 4) ((f32x2*)(ws + WS_STAT))[r0 + lane] = f32x2{rb1(mm), rs};
    STAMP(0, 3);
  }
}

constexpr int CV_CS = 4160;
constexpr int CV_UOFF = 8 * CV_CS;
constexpr int CV_CH = CV_UOFF + 286 * 16 + 32;
constexpr int CV_RED = 2 * CV_CH;
constexpr int CV_PRED = CV_RED + 32768;
constexpr int K2_LDS = CV_PRED + 64 * 33 * 4;

NOPK __global__ void __launch_bounds__(512, 2) k2_conv(const u32x4* __restrict__ ssmk, const unsigned short* __restrict__ ssmD,
                                                       const u32x2* __restrict__ wgate, const u32x2* __restrict__ wup,
                                                       const u32* __restrict__ wdown, unsigned char* __restrict__ ws) {
  extern __shared__ __attribute__((aligned(16))) unsigned char smem[];
  const int b = blockIdx.x;
  if (b >= 224) {
    const int tid = threadIdx.x;
    const int pb = b - 224;
    u32x4* dst = (u32x4*)(ws + WS_WD);
    u32 wv[4][8];
#pragma unroll
    for (int it = 0; it < 4; ++it) {
      const int li = it * 512 + tid;
      const int item = pb * 1568 + (li < 1568 ? li : 0);
      const int fo = item / 224, np = item - fo * 224;
#pragma unroll
      for (int j = 0; j < 8; ++j)
        wv[it][j] = wdown[(size_t)((fo >> 1) * 16 + (fo & 1) * 4 + (j & 3) + 8 * (j >> 2)) * 224 + np];
    }
    SB();
#pragma unroll
    for (int it = 0; it < 4; ++it) {
      const int li = it * 512 + tid;
      if (li < 1568) {
        const int item = pb * 1568 + li;
        const int fo = item / 224, np = item - fo * 224;
        u32x4 o0, o1;
#pragma unroll
        for (int j = 0; j < 4; ++j) {
          o0[j] = __builtin_amdgcn_perm(wv[it][2 * j + 1], wv[it][2 * j], 0x05040100u);
          o1[j] = __builtin_amdgcn_perm(wv[it][2 * j + 1], wv[it][2 * j], 0x07060302u);
        }
        const int nn = 2 * np;
        const int fs = fo >> 1;
        u32x4* p = dst + (((size_t)(nn >> 6) * 112 + (fs & 7) * 14 + (fs >> 3)) * 2 + ((nn >> 5) & 1)) * 64 + (fo & 1) * 32 + (nn & 31);
        p[0] = o0;
        p[1] = o1;
      }
    }
    STAMP(1, 7);
    return;
  }
  const float* __restrict__ mod = (const float*)(ws + WS_MOD);
  const int w = __builtin_amdgcn_readfirstlane(threadIdx.x >> 6);
  const int unit = (b & 7) * 28 + (b >> 3);
  const int d0 = 2 * unit;
  const int lane = threadIdx.x & 63;
  const int n = lane & 15, g = lane >> 4;
  const u32x4* __restrict__ ux = (const u32x4*)(ws + WS_UX) + (size_t)unit * 1024;
  const int t0 = 256 * w + 16 * n + 4 * g;
  u32x2 xe[2];
  f32x4 st[2];
  f32x4* red = (f32x4*)(smem + CV_RED);
  STAMP(1, 0);
  f32x2 SA = *(const f32x2*)(mod + d0);
  f32x2 CAr = *(const f32x2*)(mod + D_ + d0);
  f32x2 GA = *(const f32x2*)(mod + 2 * D_ + d0);
  u32 DDr = *(const u32*)(ssmD + d0);
  SB();
  if (w >= 4) {
    const int tid = threadIdx.x - 256;
    const int mat = unit / 112, ft = unit % 112;
    const u32x2* __restrict__ W = mat ? wup : wgate;
    float* pred = (float*)(smem + CV_PRED);
    const int fq = tid & 3, ko = tid >> 2;
    const int kc = ko < 56 ? ko : 55;
    u32x2 wv[8];
    const f32x4 cbA = *(const f32x4*)(mod + 4 * D_ + kc * 8), cbB = *(const f32x4*)(mod + 4 * D_ + kc * 8 + 4);
    const f32x4 sbA = *(const f32x4*)(mod + 3 * D_ + kc * 8), sbB = *(const f32x4*)(mod + 3 * D_ + kc * 8 + 4);
#pragma unroll
    for (int j = 0; j < 8; ++j) wv[j] = W[(size_t)(kc * 8 + j) * 448 + ft * 4 + fq];
    SB();
#pragma unroll
    for (int cc = 0; cc < 2; ++cc) xe[cc] = ((const u32x2*)(ux + (t0 >> 5) * 16 + cc * 8 + 4 + ((t0 >> 3) & 3)))[g & 1];
    st[0] = ((const f32x4*)(ws + WS_STAT))[t0 / 2];
    st[1] = ((const f32x4*)(ws + WS_STAT))[t0 / 2 + 1];
    SB();
    float sc[8], sb[8];
#pragma unroll
    for (int j = 0; j < 4; ++j) {
      sc[j] = rb1(1.0f + cbA[j]);
      sc[j + 4] = rb1(1.0f + cbB[j]);
      sb[j] = sbA[j];
      sb[j + 4] = sbB[j];
    }
    float c1[4] = {0.f, 0.f, 0.f, 0.f}, c2[4] = {0.f, 0.f, 0.f, 0.f};
    u32x4 o[4];
#pragma unroll
    for (int jj = 0; jj < 4; ++jj) {
      const float a[4] = {bflo(wv[2 * jj].x), bfhi(wv[2 * jj].x), bflo(wv[2 * jj].y), bfhi(wv[2 * jj].y)};
      const float bb[4] = {bflo(wv[2 * jj + 1].x), bfhi(wv[2 * jj + 1].x), bflo(wv[2 * jj + 1].y), bfhi(wv[2 * jj + 1].y)};
#pragma unroll
      for (int f = 0; f < 4; ++f) {
        const u32 pp = pk2(a[f] * sc[2 * jj], bb[f] * sc[2 * jj + 1]);
        o[f][jj] = pp;
        c1[f] = dot2(pp, 0x3f803f80u, c1[f]);
        c2[f] = __builtin_fmaf(a[f], sb[2 * jj], c2[f]);
        c2[f] = __builtin_fmaf(bb[f], sb[2 * jj + 1], c2[f]);
      }
    }
    if (ko < 56) {
      u32x4* dst = (u32x4*)(ws + WS_WGU) + ((size_t)ft * 28 + (ko >> 1)) * 64 + (ko & 1) * 32 + mat * 16 + fq * 4;
#pragma unroll
      for (int f = 0; f < 4; ++f) dst[f] = o[f];
    }
#pragma unroll
    for (int f = 0; f < 4; ++f) {
      pred[ko * 33 + fq * 4 + f] = ko < 56 ? c1[f] : 0.f;
      pred[ko * 33 + 16 + fq * 4 + f] = ko < 56 ? c2[f] : 0.f;
    }
    STAMP(1, 1);
    bar();
    const int col = tid >> 3, part = tid & 7;
    float s = 0.f;
#pragma unroll
    for (int i = 0; i < 8; ++i) s += pred[(part * 8 + i) * 33 + col];
    s += dppf<0xB1>(s);
    s += dppf<0x4E>(s);
    s += dppf<0x141>(s);
    if (part == 0) ((float*)(ws + WS_C12))[(mat * 2 + (col >> 4)) * F_ + ft * 16 + (col & 15)] = s;
  } else {
    const int ch = w >> 1;
    const int i = threadIdx.x & 127;
    unsigned char* cb = smem + ch * CV_CH;
    const u32x4* kr = ssmk + (size_t)(d0 + ch) * 256;
    u32x4 ka[2], kb[2], uu[2];
    ka[0] = kr[i > 0 ? i - 1 : 0];
    kb[0] = kr[i];
    ka[1] = kr[i + 127];
    kb[1] = kr[i + 128];
    uu[0] = ux[(i >> 2) * 16 + ch * 8 + (i & 3)];
    uu[1] = ux[(32 + (i >> 2)) * 16 + ch * 8 + (i & 3)];
    SB();
#pragma unroll
    for (int cc = 0; cc < 2; ++cc) xe[cc] = ((const u32x2*)(ux + (t0 >> 5) * 16 + cc * 8 + 4 + ((t0 >> 3) & 3)))[g & 1];
    st[0] = ((const f32x4*)(ws + WS_STAT))[t0 / 2];
    st[1] = ((const f32x4*)(ws + WS_STAT))[t0 / 2 + 1];
    SB();
    if (i < 38) {
      unsigned char* zp = i < 8 ? cb + i * CV_CS : cb + CV_UOFF + (256 + i - 8) * 16;
      *(u32x4*)zp = u32x4{0u, 0u, 0u, 0u};
    }
    if (i == 0) ka[0] = u32x4{0u, 0u, 0u, 0u};
#pragma unroll
    for (int it = 0; it < 2; ++it) {
      const int qp = i + 1 + 128 * it;
      const u32 Dw[8] = {ka[it].x, ka[it].y, ka[it].z, ka[it].w, kb[it].x, kb[it].y, kb[it].z, kb[it].w};
      u32 E[7];
#pragma unroll
      for (int j = 0; j < 7; ++j) E[j] = __builtin_amdgcn_alignbit(Dw[j + 1], Dw[j], 16);
      unsigned char* p = cb + qp * 16;
#pragma unroll
      for (int s2 = 0; s2 < 4; ++s2) {
        *(u32x4*)(p + (2 * s2) * CV_CS) = u32x4{E[s2], E[s2 + 1], E[s2 + 2], E[s2 + 3]};
        *(u32x4*)(p + (2 * s2 + 1) * CV_CS) = u32x4{Dw[s2 + 1], Dw[s2 + 2], Dw[s2 + 3], Dw[s2 + 4]};
      }
    }
#pragma unroll
    for (int it = 0; it < 2; ++it) *(u32x4*)(cb + CV_UOFF + (255 - (i + 128 * it)) * 16) = uu[it];
    STAMP(1, 2);
    bar();
    STAMP(1, 3);

    const int sh = w & 1;
    const unsigned char* ap = cb + (n & 7) * CV_CS + ((n >> 3) + g) * 16 + sh * 256;
    const unsigned char* bp = cb + CV_UOFF + (254 - 2 * n + g) * 16 - 3584 + sh * 256;
    f32x4 acc[8];
#pragma unroll
    for (int c = 0; c < 8; ++c) acc[c] = f32x4{0.f, 0.f, 0.f, 0.f};
    u32x4 A[2][8], B[2][8];
#pragma unroll
    for (int e = 0; e < 8; ++e) {
      A[0][e] = *(const u32x4*)(ap + 512 * e);
      B[0][e] = *(const u32x4*)(bp + 512 * (7 - e));
      SB();
    }
#pragma unroll
    for (int s = 0; s < 4; ++s) {
      const int cu = s & 1, nx = cu ^ 1;
      int cnt = 0;
#pragma unroll
      for (int c = 0; c < 8; ++c) {
#pragma unroll
        for (int ii = 0; ii <= c; ++ii) {
          acc[c] = mfma16(A[cu][ii], B[cu][c - ii], acc[c]);
          if (s < 3 && cnt >= 2 && cnt < 34 && (cnt & 1) == 0) {
            const int e = (cnt - 2) >> 2;
            if (((cnt - 2) >> 1) & 1) B[nx][e] = *(const u32x4*)(bp + 64 * (s + 1) + 512 * (7 - e));
            else A[nx][e] = *(const u32x4*)(ap + 64 * (s + 1) + 512 * e);
            SB();
          }
          ++cnt;
        }
        if (s == 3) { red[(w * 8 + c) * 64 + lane] = acc[c]; SB(); }
      }
      SB();
    }
    STAMP(1, 4);
  }
  bar();
  STAMP(1, 5);
  {
    const f32x2 CA = rb2(CAr + 1.0f);
    const f32x2 DD = up2(DDr);
    u32* x1f = (u32*)(ws + WS_X1F);
    const int ks = d0 >> 4, hk = (d0 >> 3) & 1, jj = (d0 & 7) >> 1;
    const f32x4 y0 = red[(0 * 8 + w) * 64 + lane] + red[(1 * 8 + w) * 64 + lane];
    const f32x4 y1 = red[(2 * 8 + w) * 64 + lane] + red[(3 * 8 + w) * 64 + lane];
    const int uo = CV_UOFF + (255 - (t0 >> 3)) * 16 + (1 - (g & 1)) * 8;
    const u32x2 ur0 = *(const u32x2*)(smem + uo);
    const u32x2 ur1 = *(const u32x2*)(smem + CV_CH + uo);
    const float u0[4] = {bfhi(ur0.y), bflo(ur0.y), bfhi(ur0.x), bflo(ur0.x)};
    const float u1[4] = {bfhi(ur1.y), bflo(ur1.y), bfhi(ur1.x), bflo(ur1.x)};
    const float xa[4] = {bflo(xe[0].x), bfhi(xe[0].x), bflo(xe[0].y), bfhi(xe[0].y)};
    const float xb[4] = {bflo(xe[1].x), bfhi(xe[1].x), bflo(xe[1].y), bfhi(xe[1].y)};
    const float mn[4] = {st[0].x, st[0].z, st[1].x, st[1].z};
    const float rs[4] = {st[0].y, st[0].w, st[1].y, st[1].w};
    u32* dst = x1f + (((size_t)(8 * w + 4 * (g >> 1)) * 28 + ks) * 64 + hk * 32 + (g & 1) * 16 + n) * 4 + jj;
#pragma unroll
    for (int r = 0; r < 4; ++r) {
      const float h0 = __builtin_fmaf(xa[r] - mn[r], rs[r] * CA.x, SA.x);
      const float h1 = __builtin_fmaf(xb[r] - mn[r], rs[r] * CA.y, SA.y);
      const float z0 = __builtin_fmaf(u0[r], DD.x, y0[r]);
      const float z1 = __builtin_fmaf(u1[r], DD.y, y1[r]);
      dst[(size_t)r * 28 * 256] = pk2(__builtin_fmaf(h0 * GA.x, z0, xa[r]), __builtin_fmaf(h1 * GA.y, z1, xb[r]));
    }
  }
  STAMP(1, 6);
}

constexpr int K3_ST = 131072;
constexpr int K3_SY = K3_ST + 1024;
constexpr int K3_LDS = K3_SY + 64;
constexpr int K3_PF = 12;
constexpr int AUX_SC1 = 16;
constexpr unsigned SPIN_CAP = 1u << 18;

typedef __attribute__((address_space(1))) u32 gu32;
typedef __attribute__((address_space(3))) u32 lu32;
DEV u32x4 bld(__amdgpu_buffer_rsrc_t rs, int voff, int soff) { return __builtin_amdgcn_raw_buffer_load_b128(rs, voff, soff, 0); }
DEV u32x4 bld_sc1(__amdgpu_buffer_rsrc_t rs, int voff, int soff) { return __builtin_amdgcn_raw_buffer_load_b128(rs, voff, soff, AUX_SC1); }
DEV u32 flag_ld(const unsigned char* ws, int idx) {
  return __hip_atomic_load((gu32*)(ws + WS_FLG) + idx, __ATOMIC_RELAXED, __HIP_MEMORY_SCOPE_AGENT);
}
DEV void flag_wait(const unsigned char* ws, int idx) {
#pragma nounroll
  for (unsigned spins = 0; spins < SPIN_CAP; ++spins) {
    const u32 v = flag_ld(ws, idx);
    if (__builtin_amdgcn_ballot_w64(v == 0u) == 0ull) break;
    __builtin_amdgcn_s_sleep(1);
  }
  __builtin_amdgcn_fence(__ATOMIC_ACQUIRE, "wavefront");
}
DEV void lds_wait(lu32* p, u32 want) {
#pragma nounroll
  for (unsigned spins = 0; spins < (SPIN_CAP << 4); ++spins) {
    const u32 v = __hip_atomic_load(p, __ATOMIC_RELAXED, __HIP_MEMORY_SCOPE_WORKGROUP);
    if (__builtin_amdgcn_ballot_w64(v < want) == 0ull) break;
    __builtin_amdgcn_s_sleep(1);
  }
  __builtin_amdgcn_fence(__ATOMIC_ACQUIRE, "workgroup");
}

NOPK __global__ void __launch_bounds__(512, 2) k34_ffn(unsigned char* __restrict__ ws, u32x2* __restrict__ out) {
  extern __shared__ __attribute__((aligned(16))) unsigned char smem[];
  const int tid = threadIdx.x;
  const int b = blockIdx.x;
  const int xcd = b & 7, idx = b >> 3;
  STAMP(2, 0);
  const int grp = 4 * (xcd & 3) + idx / 7, mh = xcd >> 2, nt64 = idx % 7, Fs = 7 * mh + nt64;
  const int lane = tid & 63;
  const int w = __builtin_amdgcn_readfirstlane(tid >> 6);
  const bool lead = w < 4;
  const int ft = Fs * 8 + w;
  const __amdgpu_buffer_rsrc_t rs = __builtin_amdgcn_make_buffer_rsrc((void*)ws, 0, (int)WS_END, 0x00020000);
  const int vo = lane * 16;
  const int woff = (int)WS_WGU + ft * 28 * 1024;
  const int doff = (int)WS_WD + (nt64 * 112 + 14 * w) * 2048;
  const int aoff = (int)WS_ACT + ((grp * 4 + mh * 2) * 112 + w) * 1024;
  u32x4* xl = (u32x4*)smem;
  f32x2* stl = (f32x2*)(smem + K3_ST);
  lu32* sy = (lu32*)(smem + K3_SY);
  const int m32 = lane & 31, h = lane >> 5;

  u32x4 wf[28];
  if (lead) {
    __builtin_amdgcn_s_setprio(2);
    if (tid < 8) sy[tid] = 0u;
    __builtin_amdgcn_s_barrier();
#pragma unroll
    for (int k = 0; k < K3_PF; ++k) { wf[k] = bld(rs, vo, woff + k * 1024); SB(); }
    bar();
  } else {
    __builtin_amdgcn_s_setprio(3);
    const int q = w - 4;
    const int xoff = (int)WS_X1F + (grp * 4 + q) * 28 * 1024;
    u32x4 xs[28];
#pragma unroll
    for (int k = 0; k < 14; ++k) { xs[k] = bld(rs, vo, xoff + k * 1024); SB(); }
    __builtin_amdgcn_s_barrier();
#pragma unroll
    for (int k = 14; k < 28; ++k) { xs[k] = bld(rs, vo, xoff + k * 1024); SB(); }
    const u32 one = (((lane >> 4) ^ lane) & 1) ? 0u : 0x3f803f80u;
    const u32x4 pat = {one, one, one, one};
    f32x4 sm = {0.f, 0.f, 0.f, 0.f};
    float sq[4] = {0.f, 0.f, 0.f, 0.f};
#pragma unroll
    for (int k = 0; k < 14; ++k) {
      xl[(k * 4 + q) * 64 + lane] = xs[k];
      sm = mfma16(pat, xs[k], sm);
      SB();
    }
    STAMP(2, 1);
    bar();
#pragma unroll
    for (int k = 0; k < 14; ++k)
#pragma unroll
      for (int j = 0; j < 4; ++j) {
        const float lo = bflo(xs[k][j]), hi = bfhi(xs[k][j]);
        sq[j] = __builtin_fmaf(lo, lo, sq[j]);
        sq[j ^ 1] = __builtin_fmaf(hi, hi, sq[j ^ 1]);
      }
    SB();
#pragma unroll
    for (int k = 14; k < 28; ++k) {
      xl[(k * 4 + q) * 64 + lane] = xs[k];
      sm = mfma16(pat, xs[k], sm);
      SB();
    }
#pragma unroll
    for (int k = 0; k < K3_PF; ++k) { wf[k] = bld(rs, vo, woff + k * 1024); SB(); }
    bar();
#pragma unroll
    for (int k = 14; k < 28; ++k)
#pragma unroll
      for (int j = 0; j < 4; ++j) {
        const float lo = bflo(xs[k][j]), hi = bfhi(xs[k][j]);
        sq[j] = __builtin_fmaf(lo, lo, sq[j]);
        sq[j ^ 1] = __builtin_fmaf(hi, hi, sq[j ^ 1]);
      }
    float s2 = (sq[0] + sq[1]) + (sq[2] + sq[3]);
    {
      const u32 a = __builtin_bit_cast(u32, s2);
      auto r = __builtin_amdgcn_permlane32_swap(a, a, false, false);
      s2 = __builtin_bit_cast(float, (u32)r[0]) + __builtin_bit_cast(float, (u32)r[1]);
    }
    const float s1 = (lane & 16) ? sm[1] : sm[0];
    const float mean = s1 * (1.0f / D_);
    const float var = __builtin_fmaxf(__builtin_fmaf(-mean, mean, s2 * (1.0f / D_)), 0.f);
    const f32x2 v = {ln_sd(var), -rb1(mean)};
    if (lane < 32) stl[q * 32 + lane] = v;
    asm volatile("s_waitcnt lgkmcnt(0)" ::: "memory");
    if (lane == 0) __hip_atomic_fetch_add(sy + 0, 1u, __ATOMIC_RELAXED, __HIP_MEMORY_SCOPE_WORKGROUP);
    __builtin_amdgcn_s_setprio(1);
    SB();
  }
  STAMP(2, 2);

  f32x16 ac[4];
#pragma unroll
  for (int m = 0; m < 4; ++m)
#pragma unroll
    for (int r = 0; r < 16; ++r) ac[m][r] = 0.f;
  u32x4 wd[28];
  {
    const u32x4* xp = xl + lane;
    const float* c12 = (const float*)(ws + WS_C12) + ft * 16 + 4 * h;
    const int acts = (int)WS_ACT + ((grp * 4) * 112 + ft) * 1024;
    f32x4 cc1[4], cc2[4];
    u32x4 xf[2][4];
#pragma unroll
    for (int m = 0; m < 4; ++m) xf[0][m] = xp[m * 64];
    SB();
#pragma unroll
    for (int k = 0; k < 16; ++k) {
      const int cu = k & 1, nx = cu ^ 1;
      wf[k + K3_PF] = bld(rs, vo, woff + (k + K3_PF) * 1024);
      if (k == 10) {
#pragma unroll
        for (int e = 0; e < 4; ++e) {
          cc1[e] = *(const f32x4*)(c12 + (e >> 1) * 2 * F_ + (e & 1) * 8);
          cc2[e] = *(const f32x4*)(c12 + (e >> 1) * 2 * F_ + F_ + (e & 1) * 8);
        }
      }
      SB();
      if (k == 13) {
        if (lead) bar();
        SB();
      }
#pragma unroll
      for (int m = 0; m < 4; ++m) {
        if (k < 15) xf[nx][m] = xp[((k + 1) * 4 + m) * 64];
        else xf[nx][m] = xp[((16 + m) * 4 + 0) * 64];
        ac[m] = mfma32(wf[k], xf[cu][m], ac[m]);
        SB();
      }
    }
    STAMP(3, 3);
    lds_wait(sy + 0, 4u);
    f32x2 sv[4];
#pragma unroll
    for (int m = 0; m < 4; ++m) sv[m] = stl[m * 32 + m32];
    SB();
    float et[8], eq[8], K0 = 0.f, K1 = 0.f, S2 = 0.f;
#pragma unroll
    for (int m = 0; m < 5; ++m) {
#pragma unroll
      for (int j = 0; j < 12; ++j) {
        const int sidx = m * 12 + j;
        if (m < 4) {
          const u32x4 xc = xf[0][sidx & 3];
          const int nxt = sidx + 4;
          ac[m] = mfma32(wf[16 + j], xc, ac[m]);
          if (nxt < 48) xf[0][sidx & 3] = xp[((16 + nxt % 12) * 4 + nxt / 12) * 64];
          if (m >= 1 && (j & 1) == 0) wd[(m - 1) * 6 + (j >> 1)] = bld(rs, vo, doff + ((m - 1) * 6 + (j >> 1)) * 1024);
        }
        if (m == 0) {
#pragma unroll
          for (int i = j * 4; i < j * 4 + 4; ++i) {
            const int t = 1 + i / 16, r = i % 16;
            ac[t][r] = __builtin_fmaf(cc2[r >> 2][r & 3], sv[t].x, __builtin_fmaf(cc1[r >> 2][r & 3], sv[t].y, ac[t][r]));
          }
        } else {
          const int t = m - 1;
          if (j == 0) {
            const float rsd = __builtin_amdgcn_rcpf(sv[t].x);
            K0 = (-2.0f * 0.7978845608028654f * 1.4426950408889634f) * rsd;
            K1 = K0 * 0.044715f * rsd * rsd;
            S2 = sv[t].x * sv[t].x;
          }
          if (t == 0 && j < 2) {
#pragma unroll
            for (int r = j * 8; r < j * 8 + 8; ++r)
              ac[0][r] = __builtin_fmaf(cc2[r >> 2][r & 3], sv[0].x, __builtin_fmaf(cc1[r >> 2][r & 3], sv[0].y, ac[0][r]));
          }
#pragma unroll
          for (int r = 0; r < 8; ++r) {
            if (j == 2) et[r] = ac[t][r] * ac[t][r];
            if (j == 3) et[r] = __builtin_fmaf(et[r], K1, K0);
            if (j == 4) et[r] = ac[t][r] * et[r];
            if (j == 5 + (r >> 2)) et[r] = __builtin_amdgcn_exp2f(et[r]);
            if (j == 6 - (r >> 2)) eq[r] = ac[t][r] * ac[t][r + 8];
            if (j == 7) et[r] = __builtin_fmaf(et[r], S2, S2);
            if (j == 8 + (r >> 2)) et[r] = __builtin_amdgcn_rcpf(et[r]);
            if (j == 10) eq[r] = eq[r] * et[r];
          }
          if (j == 11)
            __builtin_amdgcn_raw_buffer_store_b128(u32x4{pk2(eq[0], eq[1]), pk2(eq[2], eq[3]), pk2(eq[4], eq[5]), pk2(eq[6], eq[7])},
                                                   rs, vo, acts + t * 112 * 1024, AUX_SC1);
        }
        SB();
      }
      if (m == 3) {
        STAMP(2, 3);
        if (lane == 0) __hip_atomic_fetch_add(sy + 4, 1u, __ATOMIC_RELAXED, __HIP_MEMORY_SCOPE_WORKGROUP);
        __builtin_amdgcn_s_setprio(3);
#pragma unroll
        for (int qq = 18; qq < 28; ++qq) { wd[qq] = bld(rs, vo, doff + qq * 1024); SB(); }
      }
    }
  }
  const int mr = tid >> 4, c4 = tid & 15;
  const int nn = nt64 * 64 + 4 * c4;
  u32x2 xr[2];
#pragma unroll
  for (int a = 0; a < 2; ++a)
    xr[a] = ((const u32x2*)(ws + WS_X1F))[((((size_t)(grp * 4 + mh * 2 + a) * 28 + (nn >> 4)) * 64 + ((nn >> 3) & 1) * 32 + mr) << 1) + (c4 & 1)];
  const f32x4 gb = *(const f32x4*)((const float*)(ws + WS_MOD) + 5 * D_ + nn);
  SB();
  STAMP(2, 4);
  asm volatile("s_waitcnt vmcnt(0)" ::: "memory");
  if (lane == 0) __hip_atomic_store((gu32*)(ws + WS_FLG) + (grp * 8 + w) * 16 + Fs, 1u, __ATOMIC_RELAXED, __HIP_MEMORY_SCOPE_AGENT);
  if (lead) __builtin_amdgcn_s_setprio(0);
  SB();
  STAMP(2, 5);

  flag_wait(ws, (grp * 8 + w) * 16 + (lane < 14 ? lane : 13));
  STAMP(2, 6);
  constexpr int AF = 6;
  u32x4 af[14][2];
#pragma unroll
  for (int k = 0; k < AF; ++k) {
    af[k][0] = bld_sc1(rs, vo, aoff + k * 8192);
    af[k][1] = bld_sc1(rs, vo, aoff + 112 * 1024 + k * 8192);
    SB();
  }
  f32x16 acc[2][2];
#pragma unroll
  for (int a = 0; a < 2; ++a)
#pragma unroll
    for (int c = 0; c < 2; ++c)
#pragma unroll
      for (int r = 0; r < 16; ++r) acc[a][c][r] = 0.f;
#pragma unroll
  for (int k = 0; k < 14; ++k) {
    acc[0][0] = mfma32(wd[2 * k], af[k][0], acc[0][0]);
    acc[0][1] = mfma32(wd[2 * k + 1], af[k][0], acc[0][1]);
    acc[1][0] = mfma32(wd[2 * k], af[k][1], acc[1][0]);
    acc[1][1] = mfma32(wd[2 * k + 1], af[k][1], acc[1][1]);
    SB();
#pragma unroll
    for (int e = 0; e < 2; ++e) {
      const int kk = AF + 2 * k + e;
      if (kk < 14) {
        af[kk][0] = bld_sc1(rs, vo, aoff + kk * 8192);
        af[kk][1] = bld_sc1(rs, vo, aoff + 112 * 1024 + kk * 8192);
      }
    }
    SB();
  }
  STAMP(2, 7);
  lds_wait(sy + 4, 8u);
  {
    f32x4* red = (f32x4*)smem;
#pragma unroll
    for (int a = 0; a < 2; ++a)
#pragma unroll
      for (int nt = 0; nt < 2; ++nt)
#pragma unroll
        for (int q = 0; q < 4; ++q) {
          const int cc = nt * 8 + q * 2 + h;
          red[(w * 2 + a) * 512 + cc * 32 + (m32 ^ cc)] =
              f32x4{acc[a][nt][4 * q], acc[a][nt][4 * q + 1], acc[a][nt][4 * q + 2], acc[a][nt][4 * q + 3]};
        }
    STAMP(3, 0);
    bar();
    STAMP(3, 1);
#pragma unroll
    for (int a = 0; a < 2; ++a) {
      f32x4 pv[8];
#pragma unroll
      for (int ww = 0; ww < 8; ++ww) pv[ww] = red[(ww * 2 + a) * 512 + c4 * 32 + (mr ^ c4)];
      const f32x4 s = ((pv[0] + pv[1]) + (pv[2] + pv[3])) + ((pv[4] + pv[5]) + (pv[6] + pv[7]));
      const int mu = grp * 128 + mh * 64 + a * 32 + mr;
      const int t = (mu & ~255) | ((mu & 15) << 4) | ((mu >> 4) & 8) | ((mu >> 2) & 4) | ((mu >> 5) & 3);
      const float xv[4] = {bflo(xr[a].x), bfhi(xr[a].x), bflo(xr[a].y), bfhi(xr[a].y)};
      float o[4];
#pragma unroll
      for (int r = 0; r < 4; ++r) o[r] = __builtin_fmaf(gb[r], s[r], xv[r]);
      out[((size_t)t * D_ + nn) >> 2] = u32x2{pk2(o[0], o[1]), pk2(o[2], o[3])};
    }
  }
  STAMP(3, 2);
}

extern "C" void kernel_launch(void* const* d_in, const int* in_sizes, int n_in, void* d_out, int out_size, void* d_ws,
                              size_t ws_size, hipStream_t stream) {
  (void)in_sizes; (void)n_in; (void)out_size; (void)ws_size;
  const void* A_q = d_in[0];
  const void* A_k = d_in[1];
  const void* A_v = d_in[2];
  const void* A_shift = d_in[3];
  const void* A_ssmk = d_in[4];
  const void* A_ssmD = d_in[5];
  const void* W_gate = d_in[7];
  const void* W_up = d_in[8];
  const void* W_down = d_in[9];
  const void* cvec = d_in[10];
  const void* w_mod = d_in[11];
  unsigned char* ws = (unsigned char*)d_ws;

  (void)hipFuncSetAttribute((const void*)k2_conv, hipFuncAttributeMaxDynamicSharedMemorySize, K2_LDS);
  (void)hipFuncSetAttribute((const void*)k34_ffn, hipFuncAttributeMaxDynamicSharedMemorySize, K3_LDS);

  k1_prep<<<K1_TR + K1_MOD + K1_ST, 256, 0, stream>>>((const u32*)A_q, (const u32*)A_k, (const u32*)A_v,
                                                       (const u32x4*)A_shift, (const unsigned short*)cvec,
                                                       (const u32x4*)w_mod, ws);
  k2_conv<<<256, 512, K2_LDS, stream>>>((const u32x4*)A_ssmk, (const unsigned short*)A_ssmD,
                                        (const u32x2*)W_gate, (const u32x2*)W_up, (const u32*)W_down, ws);
  k34_ffn<<<224, 512, K3_LDS, stream>>>(ws, (u32x2*)d_out);
}
```

```cpp
#include <hip/hip_runtime.h>
#include <hip/hip_bf16.h>

typedef unsigned int u32;
typedef u32 u32x2 __attribute__((ext_vector_type(2)));
typedef u32 u32x4 __attribute__((ext_vector_type(4)));
typedef float f32x2 __attribute__((ext_vector_type(2)));
typedef float f32x4 __attribute__((ext_vector_type(4)));
typedef float f32x16 __attribute__((ext_vector_type(16)));
typedef __bf16 bf16x2 __attribute__((ext_vector_type(2)));
typedef __bf16 bf16x8 __attribute__((ext_vector_type(8)));

#if defined(__HIP_DEVICE_COMPILE__)
#define NOPK __attribute__((target("no-packed-fp32-ops")))
#else
#define NOPK
#endif
#define DEV NOPK __device__ __forceinline__
#define SB() __builtin_amdgcn_sched_barrier(0)

#define STAMP(kid, slot)
#define STAMPB(kid, slot)

constexpr int S_ = 2048, D_ = 448, F_ = 1792;
constexpr float EPS_ = 1e-5f;

constexpr size_t WS_MOD = 0;
constexpr size_t WS_STAT = 49152;
constexpr size_t WS_FLG = 65536;
constexpr size_t WS_C12 = 81920;
constexpr size_t WS_UX = 196608;
constexpr size_t WS_X1F = WS_UX + (size_t)224 * 16384;
constexpr size_t WS_WGU = WS_X1F + (size_t)128 * 14 * 1024;
constexpr size_t WS_WD = WS_WGU + (size_t)112 * 2 * 14 * 1024;
constexpr size_t WS_ACT = WS_WD + (size_t)14 * 112 * 1024;
constexpr size_t WS_END = WS_ACT + (size_t)64 * 112 * 1024;

DEV float bflo(u32 v) { return __builtin_bit_cast(float, v << 16); }
DEV float bfhi(u32 v) { return __builtin_bit_cast(float, v & 0xffff0000u); }
DEV u32 pk2(float a, float b) {
  f32x2 v = {a, b};
  bf16x2 r = __builtin_convertvector(v, bf16x2);
  return __builtin_bit_cast(u32, r);
}
DEV f32x2 up2(u32 v) { return f32x2{bflo(v), bfhi(v)}; }
DEV f32x2 rb2(f32x2 v) { return up2(pk2(v.x, v.y)); }
DEV float rb1(float a) { return bflo(pk2(a, a)); }
DEV float bf2f(unsigned short b) { return __builtin_bit_cast(float, (u32)b << 16); }

DEV f32x4 mfma16(u32x4 a, u32x4 b, f32x4 c) {
  return __builtin_amdgcn_mfma_f32_16x16x32_bf16(__builtin_bit_cast(bf16x8, a), __builtin_bit_cast(bf16x8, b), c, 0, 0, 0);
}
DEV f32x16 mfma32(u32x4 a, u32x4 b, f32x16 c) {
  return __builtin_amdgcn_mfma_f32_32x32x16_bf16(__builtin_bit_cast(bf16x8, a), __builtin_bit_cast(bf16x8, b), c, 0, 0, 0);
}
DEV float dot2(u32 a, u32 b, float c) {
  return __builtin_amdgcn_fdot2_f32_bf16(__builtin_bit_cast(bf16x2, a), __builtin_bit_cast(bf16x2, b), c, false);
}
template <int CTRL> DEV float dppf(float v) {
  return __builtin_bit_cast(float, __builtin_amdgcn_update_dpp(0, __builtin_bit_cast(int, v), CTRL, 0xf, 0xf, true));
}
DEV float row16_sum(float v) {
  v += dppf<0xB1>(v);
  v += dppf<0x4E>(v);
  v += dppf<0x141>(v);
  v += dppf<0x140>(v);
  return v;
}
DEV float rdlane(float v, int l) {
  return __builtin_bit_cast(float, __builtin_amdgcn_readlane(__builtin_bit_cast(int, v), l));
}
DEV float wave_sum(float v) {
  v = row16_sum(v);
  return (rdlane(v, 0) + rdlane(v, 16)) + (rdlane(v, 32) + rdlane(v, 48));
}
DEV float col4_sum(float v) {
  u32 a = __builtin_bit_cast(u32, v);
  auto r = __builtin_amdgcn_permlane16_swap(a, a, false, false);
  v = __builtin_bit_cast(float, (u32)r[0]) + __builtin_bit_cast(float, (u32)r[1]);
  a = __builtin_bit_cast(u32, v);
  auto q = __builtin_amdgcn_permlane32_swap(a, a, false, false);
  return __builtin_bit_cast(float, (u32)q[0]) + __builtin_bit_cast(float, (u32)q[1]);
}
DEV void bar() {
  asm volatile("s_waitcnt lgkmcnt(0)" ::: "memory");
  __builtin_amdgcn_s_barrier();
  asm volatile("" ::: "memory");
}
typedef __attribute__((address_space(3))) u32 lu32;
DEV void lds_arrive(lu32* p) {
  asm volatile("s_waitcnt lgkmcnt(0)" ::: "memory");
  if ((threadIdx.x & 63) == 0) __hip_atomic_fetch_add(p, 1u, __ATOMIC_RELAXED, __HIP_MEMORY_SCOPE_WORKGROUP);
}
DEV void lds_wait(lu32* p, u32 want) {
#pragma nounroll
  for (unsigned spins = 0; spins < (1u << 22); ++spins) {
    const u32 v = __hip_atomic_load(p, __ATOMIC_RELAXED, __HIP_MEMORY_SCOPE_WORKGROUP);
    if (__builtin_amdgcn_readfirstlane(v) >= want) break;
    __builtin_amdgcn_s_sleep(1);
  }
  asm volatile("" ::: "memory");
}
DEV float ln_sd(float var) { return rb1(__builtin_amdgcn_sqrtf(rb1(rb1(var) + EPS_))); }
DEV float ln_rsd(float var) { return __builtin_amdgcn_rcpf(ln_sd(var)); }
DEV void rows_bcast(float v, float (&o)[4]) {
  const u32 a = __builtin_bit_cast(u32, v);
  auto r = __builtin_amdgcn_permlane16_swap(a, a, false, false);
  auto e = __builtin_amdgcn_permlane32_swap((u32)r[0], (u32)r[0], false, false);
  auto d = __builtin_amdgcn_permlane32_swap((u32)r[1], (u32)r[1], false, false);
  o[0] = __builtin_bit_cast(float, (u32)e[0]);
  o[2] = __builtin_bit_cast(float, (u32)e[1]);
  o[1] = __builtin_bit_cast(float, (u32)d[0]);
  o[3] = __builtin_bit_cast(float, (u32)d[1]);
}

constexpr int K1_TR = 224;
constexpr int K1_MOD = 168;
constexpr int K1_ST = 128;

NOPK __global__ void __launch_bounds__(256) k1_prep(const u32* __restrict__ xq, const u32* __restrict__ kin,
                                                    const u32* __restrict__ vin, const u32x4* __restrict__ shiftk,
                                                    const unsigned short* __restrict__ cvec,
                                                    const u32x4* __restrict__ wmod, unsigned char* __restrict__ ws) {
  __shared__ float red[32 * 65];
  asm volatile("" ::"s"(xq), "s"(kin), "s"(vin), "s"(shiftk), "s"(cvec), "s"(wmod), "s"(ws));
  const int tid = threadIdx.x;
  const int b = blockIdx.x;
  STAMPB(0, 0);
  if (b < K1_TR) {
    const int rg = b / 7, cg = b - rg * 7;
    const int p = cg * 32 + (tid & 31), j = tid >> 5;
    const int t0 = rg * 64 + j * 8;
    u32 kk[11], vv[8], xx[8];
#pragma unroll
    for (int r = 0; r < 11; ++r) {
      const int t = t0 - 3 + r;
      kk[r] = kin[(size_t)(t < 0 ? 0 : t) * 224 + p];
    }
#pragma unroll
    for (int r = 0; r < 8; ++r) vv[r] = vin[(size_t)(t0 + r) * 224 + p];
    const u32x4 sw = shiftk[p];
#pragma unroll
    for (int r = 0; r < 8; ++r) xx[r] = xq[(size_t)(t0 + r) * 224 + p];
    SB();
    if (t0 == 0) { kk[0] = 0u; kk[1] = 0u; kk[2] = 0u; }
    const f32x2 w0 = {bflo(sw.x), bflo(sw.z)}, w1 = {bfhi(sw.x), bfhi(sw.z)};
    const f32x2 w2 = {bflo(sw.y), bflo(sw.w)}, w3 = {bfhi(sw.y), bfhi(sw.w)};
    f32x2 kf[11];
#pragma unroll
    for (int r = 0; r < 11; ++r) kf[r] = up2(kk[r]);
    u32 kv[8];
#pragma unroll
    for (int r = 0; r < 8; ++r) {
      float a0 = kf[r].x * w3.x, a1 = kf[r].y * w3.y;
      a0 = __builtin_fmaf(kf[r + 1].x, w2.x, a0); a1 = __builtin_fmaf(kf[r + 1].y, w2.y, a1);
      a0 = __builtin_fmaf(kf[r + 2].x, w1.x, a0); a1 = __builtin_fmaf(kf[r + 2].y, w1.y, a1);
      a0 = __builtin_fmaf(kf[r + 3].x, w0.x, a0); a1 = __builtin_fmaf(kf[r + 3].y, w0.y, a1);
      kv[r] = pk2(a0 * bflo(vv[r]), a1 * bfhi(vv[r]));
    }
    u32x4 u0, u1, x0, x1;
#pragma unroll
    for (int i = 0; i < 4; ++i) {
      u0[i] = __builtin_amdgcn_perm(kv[6 - 2 * i], kv[7 - 2 * i], 0x05040100u);
      u1[i] = __builtin_amdgcn_perm(kv[6 - 2 * i], kv[7 - 2 * i], 0x07060302u);
      x0[i] = __builtin_amdgcn_perm(xx[2 * i + 1], xx[2 * i], 0x05040100u);
      x1[i] = __builtin_amdgcn_perm(xx[2 * i + 1], xx[2 * i], 0x07060302u);
    }
    u32x4* dst = (u32x4*)(ws + WS_UX) + ((size_t)p * 64 + rg * 2 + (j >> 2)) * 16 + (j & 3);
    dst[0] = u0;
    dst[8] = u1;
    dst[4] = x0;
    dst[12] = x1;
    STAMPB(0, 2);
    return;
  }
  if (b < K1_TR + K1_MOD) {
    const int mb = b - K1_TR;
    const int cgp = mb >> 2, rq = mb & 3;
    const int c8 = tid & 7, rg = tid >> 3;
    u32x4 w[4];
    unsigned short cs[4];
#pragma unroll
    for (int i = 0; i < 4; ++i) {
      const int rr = rg + 32 * i;
      w[i] = wmod[(size_t)(rq * 112 + (rr < 112 ? rr : 0)) * 336 + cgp * 8 + c8];
    }
#pragma unroll
    for (int i = 0; i < 4; ++i) {
      const int rr = rg + 32 * i;
      cs[i] = cvec[rq * 112 + (rr < 112 ? rr : 0)];
    }
    SB();
    float acc[8];
#pragma unroll
    for (int j = 0; j < 8; ++j) acc[j] = 0.f;
#pragma unroll
    for (int i = 0; i < 4; ++i) {
      const float c = bf2f(cs[i]);
      float sc = rb1(c * __builtin_amdgcn_rcpf(1.0f + __builtin_amdgcn_exp2f(-1.4426950408889634f * c)));
      if (rg + 32 * i >= 112) sc = 0.f;
#pragma unroll
      for (int j = 0; j < 4; ++j) {
        acc[2 * j] = __builtin_fmaf(sc, bflo(w[i][j]), acc[2 * j]);
        acc[2 * j + 1] = __builtin_fmaf(sc, bfhi(w[i][j]), acc[2 * j + 1]);
      }
    }
#pragma unroll
    for (int j = 0; j < 8; ++j) red[rg * 65 + c8 * 8 + j] = acc[j];
    bar();
    const int col = tid >> 2, part = tid & 3;
    float s = 0.f;
#pragma unroll
    for (int i = 0; i < 8; ++i) s += red[(part * 8 + i) * 65 + col];
    s += dppf<0xB1>(s);
    s += dppf<0x4E>(s);
    if (part == 0) ((float*)(ws + WS_MOD))[rq * 2688 + cgp * 64 + col] = s;
    STAMPB(0, 1);
    return;
  }
  {
    const int sb = b - K1_MOD - K1_TR;
    const int lane = tid & 63, w = tid >> 6;
    const int r0 = sb * 16 + w * 4;
    const int lc = lane < 56 ? lane : 55;
    if (tid < 32) ((u32*)(ws + WS_FLG))[sb * 32 + tid] = 0u;
    u32x4 xv[4];
#pragma unroll
    for (int r = 0; r < 4; ++r) xv[r] = ((const u32x4*)xq)[(size_t)(r0 + r) * 56 + lc];
    SB();
    float mean[4], var[4];
#pragma unroll
    for (int r = 0; r < 4; ++r) {
      float s = 0.f;
#pragma unroll
      for (int j = 0; j < 4; ++j) s += bflo(xv[r][j]) + bfhi(xv[r][j]);
      if (lane >= 56) s = 0.f;
      mean[r] = wave_sum(s) * (1.0f / D_);
    }
#pragma unroll
    for (int r = 0; r < 4; ++r) {
      float q = 0.f;
#pragma unroll
      for (int j = 0; j < 4; ++j) {
        const float a = bflo(xv[r][j]) - mean[r], bb = bfhi(xv[r][j]) - mean[r];
        q = __builtin_fmaf(a, a, q);
        q = __builtin_fmaf(bb, bb, q);
      }
      if (lane >= 56) q = 0.f;
      var[r] = wave_sum(q) * (1.0f / D_);
    }
    const float mm = lane == 0 ? mean[0] : lane == 1 ? mean[1] : lane == 2 ? mean[2] : mean[3];
    const float vv = lane == 0 ? var[0] : lane == 1 ? var[1] : lane == 2 ? var[2] : var[3];
    const float rs = ln_rsd(vv);
    if (lane < 4) ((f32x2*)(ws + WS_STAT))[r0 + lane] = f32x2{rb1(mm), rs};
    STAMPB(0, 3);
  }
}

constexpr int CV_CS = 4160;
constexpr int CV_UOFF = 4 * CV_CS;
constexpr int CV_CH = CV_UOFF + 286 * 16 + 32;
constexpr int CV_RED = 2 * CV_CH;
constexpr int K2_LDS = CV_RED + 32768;
constexpr int K2_CONV = 224, K2_PREP = 224;

NOPK __global__ void __launch_bounds__(256, 2) k2_conv(const u32x4* __restrict__ ssmk, const unsigned short* __restrict__ ssmD,
                                                       const u32x2* __restrict__ wgate, const u32x2* __restrict__ wup,
                                                       unsigned char* __restrict__ ws) {
  extern __shared__ __attribute__((aligned(16))) unsigned char smem[];
  asm volatile("" ::"s"(ssmk), "s"(ssmD), "s"(wgate), "s"(wup), "s"(ws));
  const int b = blockIdx.x;
  const int tid = threadIdx.x;
  const float* __restrict__ mod = (const float*)(ws + WS_MOD);
  const int w = __builtin_amdgcn_readfirstlane(tid >> 6);
  const int lane = tid & 63;
  STAMPB(1, 0);
  if (b >= K2_CONV) {
    const int unit = b - K2_CONV;
    const int mat = unit / 112, fg = (unit % 112) >> 2, kq = unit & 3;
    const u32x2* __restrict__ W = mat ? wup : wgate;
    float* pm = (float*)smem + w * 64;
    f32x4* pred = (f32x4*)(smem + 1024);
    const int fq = lane & 15, ko = w * 4 + (lane >> 4);
    const int kc = ko < 14 ? ko : 13;
    u32x2 wv[8];
    float mq[4];
    {
      const int kl = w * 32 + (lane & 31);
      const int mi = (lane < 32 ? 4 * D_ : 3 * D_) + kq * 112 + (kl < 112 ? kl : 111);
#pragma unroll
      for (int q = 0; q < 4; ++q) mq[q] = mod[q * 2688 + mi];
    }
#pragma unroll
    for (int j = 0; j < 8; ++j) wv[j] = W[(size_t)(kq * 112 + kc * 8 + j) * 448 + fg * 16 + fq];
    SB();
    {
      float mv = rb1((mq[0] + mq[1]) + (mq[2] + mq[3]));
      if (lane < 32) mv = rb1(1.0f + mv);
      pm[lane] = mv;
    }
    const float* pk = pm + (lane >> 4) * 8;
    const f32x4 scA = *(const f32x4*)(pk), scB = *(const f32x4*)(pk + 4);
    const f32x4 sbA = *(const f32x4*)(pk + 32), sbB = *(const f32x4*)(pk + 36);
    const float sc[8] = {scA[0], scA[1], scA[2], scA[3], scB[0], scB[1], scB[2], scB[3]};
    const float sb[8] = {sbA[0], sbA[1], sbA[2], sbA[3], sbB[0], sbB[1], sbB[2], sbB[3]};
    float c1[4] = {0.f, 0.f, 0.f, 0.f}, c2[4] = {0.f, 0.f, 0.f, 0.f};
    u32x4 o[4];
#pragma unroll
    for (int jj = 0; jj < 4; ++jj) {
      const float a[4] = {bflo(wv[2 * jj].x), bfhi(wv[2 * jj].x), bflo(wv[2 * jj].y), bfhi(wv[2 * jj].y)};
      const float bb[4] = {bflo(wv[2 * jj + 1].x), bfhi(wv[2 * jj + 1].x), bflo(wv[2 * jj + 1].y), bfhi(wv[2 * jj + 1].y)};
#pragma unroll
      for (int f = 0; f < 4; ++f) {
        const u32 pp = pk2(a[f] * sc[2 * jj], bb[f] * sc[2 * jj + 1]);
        o[f][jj] = pp;
        c1[f] += bflo(pp) + bfhi(pp);
        c2[f] = __builtin_fmaf(a[f], sb[2 * jj], c2[f]);
        c2[f] = __builtin_fmaf(bb[f], sb[2 * jj + 1], c2[f]);
      }
    }
    {
      u32x4* tl = (u32x4*)(smem + 4096) + w * 288;
#pragma unroll
      for (int f = 0; f < 4; ++f) tl[((fq >> 2) * 4 + (lane >> 4)) * 18 + (fq & 3) * 4 + f] = o[f];
      const int ksl = lane >> 5;
      if (w * 2 + ksl < 7) {
        u32x4* dst = (u32x4*)(ws + WS_WGU) + ((size_t)(fg * 4) * 28 + kq * 7 + w * 2 + ksl) * 64 + mat * 32 + (lane & 31);
#pragma unroll
        for (int t = 0; t < 4; ++t) dst[(size_t)t * 28 * 64] = tl[(t * 4 + (lane >> 4)) * 18 + (lane & 15)];
      }
    }
    f32x4 s1, s2;
#pragma unroll
    for (int f = 0; f < 4; ++f) {
      s1[f] = col4_sum(ko < 14 ? c1[f] : 0.f);
      s2[f] = col4_sum(ko < 14 ? c2[f] : 0.f);
    }
    if (lane < 32) pred[w * 32 + lane] = lane < 16 ? s1 : s2;
    bar();
    if (tid < 32)
      *(f32x4*)((float*)(ws + WS_C12) + (size_t)(kq * 4 + mat * 2 + (tid >> 4)) * F_ + fg * 64 + fq * 4) =
          (pred[tid] + pred[32 + tid]) + (pred[64 + tid] + pred[96 + tid]);
    STAMPB(1, 1);
    return;
  }

  __builtin_amdgcn_s_setprio(3);
  const int unit = (b & 7) * 28 + (b >> 3);
  const int d0 = 2 * unit;
  const int n = lane & 15, g = lane >> 4;
  const u32x4* __restrict__ ux = (const u32x4*)(ws + WS_UX) + (size_t)unit * 1024;
  f32x4* red = (f32x4*)(smem + CV_RED);
  const int ch = w >> 1;
  const int i = tid & 127;
  unsigned char* cb = smem + ch * CV_CH;
  const u32x4* kr = ssmk + (size_t)(d0 + ch) * 256;
  u32x4 ka[2], kb[2], uu[2];
  ka[0] = kr[i > 0 ? i - 1 : 0];
  kb[0] = kr[i];
  ka[1] = kr[i + 127];
  kb[1] = kr[i + 128];
  uu[0] = ux[(i >> 2) * 16 + ch * 8 + (i & 3)];
  uu[1] = ux[(32 + (i >> 2)) * 16 + ch * 8 + (i & 3)];
  SB();
  if (i < 34) {
    unsigned char* zp = i < 4 ? cb + i * CV_CS : cb + CV_UOFF + (256 + i - 4) * 16;
    *(u32x4*)zp = u32x4{0u, 0u, 0u, 0u};
  }
  if (i == 0) ka[0] = u32x4{0u, 0u, 0u, 0u};
#pragma unroll
  for (int it = 0; it < 2; ++it) {
    const int qp = i + 1 + 128 * it;
    const u32 Dw[8] = {ka[it].x, ka[it].y, ka[it].z, ka[it].w, kb[it].x, kb[it].y, kb[it].z, kb[it].w};
    u32 E[7];
#pragma unroll
    for (int j = 0; j < 7; ++j) E[j] = __builtin_amdgcn_alignbit(Dw[j + 1], Dw[j], 16);
    unsigned char* p = cb + qp * 16;
#pragma unroll
    for (int s2 = 0; s2 < 2; ++s2) {
      *(u32x4*)(p + (2 * s2) * CV_CS) = u32x4{E[s2], E[s2 + 1], E[s2 + 2], E[s2 + 3]};
      *(u32x4*)(p + (2 * s2 + 1) * CV_CS) = u32x4{Dw[s2 + 1], Dw[s2 + 2], Dw[s2 + 3], Dw[s2 + 4]};
    }
    if (it == 1 && i == 127) {
#pragma unroll
      for (int s2 = 0; s2 < 2; ++s2) {
        *(u32x2*)(p + 16 + (2 * s2) * CV_CS) = u32x2{E[s2 + 4], E[s2 + 5]};
        *(u32x2*)(p + 16 + (2 * s2 + 1) * CV_CS) = u32x2{Dw[s2 + 5], Dw[s2 + 6]};
      }
    }
  }
#pragma unroll
  for (int it = 0; it < 2; ++it) *(u32x4*)(cb + CV_UOFF + (255 - (i + 128 * it)) * 16) = uu[it];
  SB();
  STAMP(1, 2);
  bar();
  STAMP(1, 3);

  const int sh = w & 1;
  const unsigned char* ap = cb + (n & 3) * CV_CS + ((n >> 2) + 2 * g) * 8 + sh * 256;
  const unsigned char* bp = cb + CV_UOFF + (254 - 2 * n + g) * 16 - 3584 + sh * 256;
  f32x4 acc[8];
#pragma unroll
  for (int c = 0; c < 8; ++c) acc[c] = f32x4{0.f, 0.f, 0.f, 0.f};
  u32x2 xe[2][2];
  f32x4 st[2][2];
  float mpv;
  u32 DDr;
  u32x2 Al[2][8], Ah[2][8];
  u32x4 B[2][8];
  typedef __attribute__((address_space(3))) const unsigned char* lcp;
  typedef __attribute__((address_space(3))) const u32x2* lcp2;
  lcp ap0 = (lcp)ap, ap1 = (lcp)ap + 8;
  asm volatile("" : "+v"(ap1));
#define LDA(buf, e, s) { Al[buf][e] = *(lcp2)(ap0 + 64 * (s) + 512 * (e)); SB(); Ah[buf][e] = *(lcp2)(ap1 + 64 * (s) + 512 * (e)); }
#define LDB(buf, e, s) { B[buf][e] = *(const u32x4*)(bp + 64 * (s) + 512 * (7 - (e))); }
  LDA(0, 0, 0);
  SB();
#pragma unroll
  for (int e = 0; e < 8; ++e) { LDB(0, e, 0); SB(); }
#pragma unroll
  for (int e = 1; e < 8; ++e) { LDA(0, e, 0); SB(); }
#pragma unroll
  for (int s = 0; s < 4; ++s) {
    const int cu = s & 1, nx = cu ^ 1;
    int cnt = 0;
#pragma unroll
    for (int ii = 0; ii < 8; ++ii) {
#pragma unroll
      for (int c = ii; c < 8; ++c) {
        acc[c] = mfma16(u32x4{Al[cu][ii].x, Al[cu][ii].y, Ah[cu][ii].x, Ah[cu][ii].y}, B[cu][c - ii], acc[c]);
        if (s < 3 && cnt >= 2 && cnt < 34 && (cnt & 1) == 0) {
          const int e = (cnt - 2) >> 1;
          if (e == 0) LDA(nx, 0, s + 1)
          else if (e <= 8) LDB(nx, e - 1, s + 1)
          else LDA(nx, e - 8, s + 1)
        }
        SB();
        ++cnt;
      }
      if (s == 3 && ii > 0) { red[(w * 8 + ii - 1) * 64 + lane] = acc[ii - 1]; SB(); }
      if (s == 1 && ii < 2) {
        const int c2 = ii;
        const int t0 = 256 * (2 * w + c2) + 16 * n + 4 * g;
#pragma unroll
        for (int cc = 0; cc < 2; ++cc) xe[c2][cc] = ((const u32x2*)(ux + (t0 >> 5) * 16 + cc * 8 + 4 + ((t0 >> 3) & 3)))[g & 1];
        st[c2][0] = ((const f32x4*)(ws + WS_STAT))[t0 / 2];
        st[c2][1] = ((const f32x4*)(ws + WS_STAT))[t0 / 2 + 1];
        SB();
      }
      if (s == 1 && ii == 2) {
        const int lc = lane < 24 ? lane : 23;
        mpv = mod[(lc & 3) * 2688 + (lc >> 3) * D_ + d0 + ((lc >> 2) & 1)];
        DDr = ((const u32*)ssmD)[unit + (lane >> 6)];
        SB();
      }
    }
  }
  red[(w * 8 + 7) * 64 + lane] = acc[7];
  mpv += dppf<0xB1>(mpv);
  mpv += dppf<0x4E>(mpv);
  mpv = rb1(mpv);
  if ((lane >> 3) == 1) mpv = rb1(mpv + 1.0f);
  STAMP(1, 4);
  bar();
  STAMP(1, 5);
  {
    const f32x2 SA = {rdlane(mpv, 0), rdlane(mpv, 4)};
    const f32x2 CA = {rdlane(mpv, 8), rdlane(mpv, 12)};
    const f32x2 GA = {rdlane(mpv, 16), rdlane(mpv, 20)};
    const f32x2 DD = up2(DDr);
    u32* x1f = (u32*)(ws + WS_X1F);
    const int ks = d0 >> 4, hk = (d0 >> 3) & 1, jj = (d0 & 7) >> 1;
#pragma unroll
    for (int c2 = 0; c2 < 2; ++c2) {
      const int ct = 2 * w + c2;
      const int t0 = 256 * ct + 16 * n + 4 * g;
      const f32x4 y0 = red[(0 * 8 + ct) * 64 + lane] + red[(1 * 8 + ct) * 64 + lane];
      const f32x4 y1 = red[(2 * 8 + ct) * 64 + lane] + red[(3 * 8 + ct) * 64 + lane];
      const int uo = CV_UOFF + (255 - (t0 >> 3)) * 16 + (1 - (g & 1)) * 8;
      const u32x2 ur0 = *(const u32x2*)(smem + uo);
      const u32x2 ur1 = *(const u32x2*)(smem + CV_CH + uo);
      const float u0[4] = {bfhi(ur0.y), bflo(ur0.y), bfhi(ur0.x), bflo(ur0.x)};
      const float u1[4] = {bfhi(ur1.y), bflo(ur1.y), bfhi(ur1.x), bflo(ur1.x)};
      const float xa[4] = {bflo(xe[c2][0].x), bfhi(xe[c2][0].x), bflo(xe[c2][0].y), bfhi(xe[c2][0].y)};
      const float xb[4] = {bflo(xe[c2][1].x), bfhi(xe[c2][1].x), bflo(xe[c2][1].y), bfhi(xe[c2][1].y)};
      const float mn[4] = {st[c2][0].x, st[c2][0].z, st[c2][1].x, st[c2][1].z};
      const float rs[4] = {st[c2][0].y, st[c2][0].w, st[c2][1].y, st[c2][1].w};
      u32* dst = x1f + (((size_t)(8 * ct + 4 * (g >> 1)) * 28 + ks) * 64 + hk * 32 + (g & 1) * 16 + n) * 4 + jj;
#pragma unroll
      for (int r = 0; r < 4; ++r) {
        const float h0 = __builtin_fmaf(xa[r] - mn[r], rs[r] * CA.x, SA.x);
        const float h1 = __builtin_fmaf(xb[r] - mn[r], rs[r] * CA.y, SA.y);
        const float z0 = __builtin_fmaf(u0[r], DD.x, y0[r]);
        const float z1 = __builtin_fmaf(u1[r], DD.y, y1[r]);
        dst[(size_t)r * 28 * 256] = pk2(__builtin_fmaf(h0 * GA.x, z0, xa[r]), __builtin_fmaf(h1 * GA.y, z1, xb[r]));
      }
    }
  }
  STAMPB(1, 6);
}

constexpr int K3_XB = 28 * 4096;
constexpr int K3_ST = K3_XB + 4096;
constexpr int K3_LDS = K3_ST + 2048;

DEV u32x4 bld(__amdgpu_buffer_rsrc_t rs, int voff, int soff) { return __builtin_amdgcn_raw_buffer_load_b128(rs, voff, soff, 0); }

NOPK __global__ void __launch_bounds__(512, 2) k3_gemm1(const u32* __restrict__ wdown, unsigned char* __restrict__ ws) {
  extern __shared__ __attribute__((aligned(16))) unsigned char smem[];
  asm volatile("" ::"s"(wdown), "s"(ws));
  const int tid = threadIdx.x;
  const int b = blockIdx.x;
  const int xcd = b & 7, idx = b >> 3;
  STAMPB(2, 0);
  if (b >= 224) {
    const int pb = b - 224;
    u32x4* dst = (u32x4*)(ws + WS_WD);
    u32 wv[4][8];
#pragma unroll
    for (int it = 0; it < 4; ++it) {
      const int li = it * 512 + tid;
      const int item = pb * 1568 + (li < 1568 ? li : 0);
      const int fo = item / 224, np = item - fo * 224;
#pragma unroll
      for (int j = 0; j < 8; ++j)
        wv[it][j] = wdown[(size_t)((fo >> 1) * 16 + (fo & 1) * 4 + (j & 3) + 8 * (j >> 2)) * 224 + np];
    }
    SB();
#pragma unroll
    for (int it = 0; it < 4; ++it) {
      const int li = it * 512 + tid;
      if (li < 1568) {
        const int item = pb * 1568 + li;
        const int fo = item / 224, np = item - fo * 224;
        u32x4 o0, o1;
#pragma unroll
        for (int j = 0; j < 4; ++j) {
          o0[j] = __builtin_amdgcn_perm(wv[it][2 * j + 1], wv[it][2 * j], 0x05040100u);
          o1[j] = __builtin_amdgcn_perm(wv[it][2 * j + 1], wv[it][2 * j], 0x07060302u);
        }
        const int nn = 2 * np;
        const int fs = fo >> 1;
        u32x4* p = dst + (((size_t)(nn >> 6) * 112 + (fs & 7) * 14 + (fs >> 3)) * 2 + ((nn >> 5) & 1)) * 64 + (fo & 1) * 32 + (nn & 31);
        p[0] = o0;
        p[1] = o1;
      }
    }
    return;
  }
  const int grp = 4 * (xcd & 3) + idx / 7, Fs = 7 * (xcd >> 2) + idx % 7;
  const int lane = tid & 63;
  const int w = __builtin_amdgcn_readfirstlane(tid >> 6);
  const int ft = Fs * 8 + w;
  const __amdgpu_buffer_rsrc_t rs = __builtin_amdgcn_make_buffer_rsrc((void*)ws, 0, (int)WS_END, 0x00020000);
  const int vo = lane * 16;
  const int woff = (int)WS_WGU + ft * 28 * 1024;
  const int vw = (((lane >> 4) & 1) * 32 + (lane >> 5) * 16 + (lane & 15)) * 16;
  const int q = w & 3, par = w >> 2;
  const int xoff = (int)WS_X1F + ((grp * 4 + q) * 28 + par) * 1024;
  u32x4* xl = (u32x4*)smem;
  u32x4* xw = xl + (par * 4 + q) * 64 + lane;
  const u32x4* xp = xl + lane;
  f32x2* stl = (f32x2*)(smem + K3_ST);
  const int m32 = lane & 31, h = lane >> 5;

  u32x4 xs[14], wf[29];
#define K3_PFW 6
  constexpr int PF = K3_PFW;
#pragma unroll
  for (int i = 0; i < 2; ++i) { xs[i] = bld(rs, vo, xoff + i * 2048); SB(); }
#pragma unroll
  for (int k = 0; k < 2; ++k) { wf[k] = bld(rs, vw, woff + k * 1024); SB(); }
  __builtin_amdgcn_s_barrier();
#pragma unroll
  for (int i = 2; i < 4; ++i) {
    wf[i] = bld(rs, vw, woff + i * 1024);
    xs[i] = bld(rs, vo, xoff + i * 2048);
    SB();
  }

  const u32 one = (((lane >> 4) ^ lane) & 1) ? 0u : 0x3f803f80u;
  const u32x4 pat = {one, one, one, one};
  f32x4 sm = {0.f, 0.f, 0.f, 0.f};
  float sq[4] = {0.f, 0.f, 0.f, 0.f};
#define XSTAT(kk)                                                           \
  {                                                                         \
    sm = mfma16(pat, xs[kk], sm);                                           \
    _Pragma("unroll") for (int j = 0; j < 4; ++j) {                         \
      const float lo = bflo(xs[kk][j]), hi = bfhi(xs[kk][j]);               \
      sq[j] = __builtin_fmaf(lo, lo, sq[j]);                                \
      sq[j ^ 1] = __builtin_fmaf(hi, hi, sq[j ^ 1]);                        \
    }                                                                       \
    asm volatile("" : "+v"(sq[0]), "+v"(sq[1]), "+v"(sq[2]), "+v"(sq[3])); \
  }
#pragma unroll
  for (int i = 0; i < 2; ++i) xw[i * 512] = xs[i];
  STAMP(2, 1);
  bar();
  STAMP(2, 2);
#pragma unroll
  for (int k = 4; k < PF; ++k) { wf[k] = bld(rs, vw, woff + k * 1024); SB(); }

  f32x16 ac[4];
#pragma unroll
  for (int m = 0; m < 4; ++m)
#pragma unroll
    for (int r = 0; r < 16; ++r) ac[m][r] = 0.f;
  u32x4 xf[2][4];
#pragma unroll
  for (int m = 0; m < 4; ++m) xf[0][m] = xp[m * 64];
  SB();
  float cq[2][4];
#pragma unroll
  for (int k = 0; k < 24; ++k) {
    const int cu = k & 1, nx = cu ^ 1, c = k >> 2;
    if (k + PF < 28) wf[k + PF] = bld(rs, vw, woff + (k + PF) * 1024);
    if ((k & 3) < 2 && 2 * c + 4 + (k & 3) < 14) xs[2 * c + 4 + (k & 3)] = bld(rs, vo, xoff + (2 * c + 4 + (k & 3)) * 2048);
    if (k == 20) {
      const float* c12 = (const float*)(ws + WS_C12) + ((m32 >> 4) * 2) * F_ + ft * 16 + (m32 & 15);
#pragma unroll
      for (int e = 0; e < 4; ++e) { cq[0][e] = c12[e * 4 * F_]; cq[1][e] = c12[e * 4 * F_ + F_]; }
    }
    SB();
    if ((k & 3) == 3) {
      if (k == 23) {
        XSTAT(12);
        XSTAT(13);
        float s2 = (sq[0] + sq[1]) + (sq[2] + sq[3]);
        const u32 a = __builtin_bit_cast(u32, s2);
        auto r = __builtin_amdgcn_permlane32_swap(a, a, false, false);
        s2 = __builtin_bit_cast(float, (u32)r[0]) + __builtin_bit_cast(float, (u32)r[1]);
        const float s1 = (lane & 16) ? sm[1] : sm[0];
        if (lane < 32) stl[par * 128 + q * 32 + lane] = f32x2{s1, s2};
      }
      bar();
      SB();
    }
#pragma unroll
    for (int m = 0; m < 4; ++m) {
      if (k < 23) xf[nx][m] = xp[((k + 1) * 4 + m) * 64];
      else xf[nx][m] = xp[((24 + m) * 4 + 0) * 64];
      ac[m] = mfma32(wf[k], xf[cu][m], ac[m]);
      if (m == 1 && (k & 3) < 2) XSTAT(2 * c + (k & 3));
      if (m == 3 && (k & 3) == 2) { xw[(2 * c + 2) * 512] = xs[2 * c + 2]; xw[(2 * c + 3) * 512] = xs[2 * c + 3]; }
      SB();
    }
  }
  STAMP(2, 3);
  {
    constexpr float GK0 = -2.0f * 0.7978845608028654f * 1.4426950408889634f;
    const int acts = (int)WS_ACT + ((grp * 4) * 112 + ft) * 1024;
    float sd[4];
    u32x4 bx[4];
    f32x2 pa[4], pb[4];
#pragma unroll
    for (int m = 0; m < 4; ++m) { pa[m] = stl[m * 32 + m32]; pb[m] = stl[128 + m * 32 + m32]; }
    SB();
    float et[8], eq[8], K0 = 0.f, K1 = 0.f, S2 = 0.f;
#pragma unroll
    for (int m = 0; m < 5; ++m) {
#pragma unroll
      for (int j = 0; j < 5; ++j) {
        if (m < 4) {
          if (j < 4) {
            const int sidx = m * 4 + j, nxt = sidx + 4;
            const u32x4 xc = xf[0][j];
            ac[m] = mfma32(wf[24 + j], xc, ac[m]);
            if (nxt < 16) xf[0][j] = xp[((24 + j) * 4 + m + 1) * 64];
          } else ac[m] = mfma32(wf[28], bx[m], ac[m]);
        }
        if (m == 0) {
          if (j < 2) {
#pragma unroll
            for (int t = 2 * j; t < 2 * j + 2; ++t) {
              const float mean = (pa[t].x + pb[t].x) * (1.0f / D_);
              const float var = __builtin_fmaxf(__builtin_fmaf(-mean, mean, (pa[t].y + pb[t].y) * (1.0f / D_)), 0.f);
              sd[t] = ln_sd(var);
              bx[t] = u32x4{h ? 0u : pk2(sd[t], sd[t]), h ? 0u : pk2(-mean, -mean), 0u, 0u};
            }
          }
          if (j == 2) {
            const float c1 = (cq[0][0] + cq[0][1]) + (cq[0][2] + cq[0][3]), c2 = (cq[1][0] + cq[1][1]) + (cq[1][2] + cq[1][3]);
            const float c1h = rb1(c1), c2h = rb1(c2);
            wf[28] = u32x4{pk2(c2h, c2 - c2h), pk2(c1h, c1 - c1h), 0u, 0u};
          }
        } else {
          const int t = m - 1;
          if (j == 0) {
            const float rsd = __builtin_amdgcn_rcpf(sd[t]);
            K0 = GK0 * rsd;
            K1 = K0 * 0.044715f * rsd * rsd;
            S2 = sd[t] * sd[t];
          }
#pragma unroll
          for (int r = 0; r < 8; ++r) if (j == 0) et[r] = ac[t][r] * ac[t][r];
#pragma unroll
          for (int r = 0; r < 8; ++r) if (j == 1) et[r] = __builtin_fmaf(et[r], K1, K0);
#pragma unroll
          for (int r = 0; r < 8; ++r) if (j == 1) et[r] = ac[t][r] * et[r];
#pragma unroll
          for (int r = 0; r < 8; ++r) if (j == 2) { et[r] = __builtin_amdgcn_exp2f(et[r]); eq[r] = ac[t][r] * ac[t][r + 8]; }
#pragma unroll
          for (int r = 0; r < 8; ++r) if (j == 3) et[r] = __builtin_fmaf(et[r], S2, S2);
#pragma unroll
          for (int r = 0; r < 8; ++r) if (j == 3) et[r] = __builtin_amdgcn_rcpf(et[r]);
#pragma unroll
          for (int r = 0; r < 8; ++r) if (j == 4) eq[r] = eq[r] * et[r];
          if (j == 4)
            __builtin_amdgcn_raw_buffer_store_b128(u32x4{pk2(eq[0], eq[1]), pk2(eq[2], eq[3]), pk2(eq[4], eq[5]), pk2(eq[6], eq[7])},
                                                   rs, vo, acts + t * 112 * 1024, 0);
        }
        SB();
      }
      if (m == 3) STAMP(2, 4);
    }
  }
  STAMPB(2, 5);
}

constexpr int K4_LDS = 131072;

NOPK __global__ void __launch_bounds__(512, 2) k4_gemm2(unsigned char* __restrict__ ws, u32x2* __restrict__ out) {
  extern __shared__ __attribute__((aligned(16))) unsigned char smem[];
  const int tid = threadIdx.x;
  const int b = blockIdx.x;
  const int xcd = b & 7, idx = b >> 3;
  STAMPB(3, 0);
  const int grp = 4 * (xcd & 3) + idx / 7, mh = xcd >> 2, nt64 = idx % 7;
  const int lane = tid & 63;
  const int w = __builtin_amdgcn_readfirstlane(tid >> 6);
  const __amdgpu_buffer_rsrc_t rs = __builtin_amdgcn_make_buffer_rsrc((void*)ws, 0, (int)WS_END, 0x00020000);
  const int vo = lane * 16;
  const int doff = (int)WS_WD + (nt64 * 112 + 14 * w) * 2048;
  const int aoff = (int)WS_ACT + ((grp * 4 + mh * 2) * 112 + w) * 1024;
  const int m32 = lane & 31, h = lane >> 5;
  constexpr int PF = 3;
  u32x4 wd[14][2], af[14][2];
#define K4_LOAD(k)                                                   \
  {                                                                  \
    af[k][0] = bld(rs, vo, aoff + (k) * 8192);                       \
    wd[k][0] = bld(rs, vo, doff + (k) * 2048);                       \
    af[k][1] = bld(rs, vo, aoff + 112 * 1024 + (k) * 8192);          \
    wd[k][1] = bld(rs, vo, doff + (k) * 2048 + 1024);                \
    SB();                                                            \
  }
#pragma unroll
  for (int k = 0; k < PF; ++k) K4_LOAD(k);
  const int mr = tid >> 4, c4 = tid & 15;
  const int nn = nt64 * 64 + 4 * c4;
  u32x2 xr[2];
  f32x4 gbp[4];
  f32x16 acc[2][2];
#pragma unroll
  for (int a = 0; a < 2; ++a)
#pragma unroll
    for (int c = 0; c < 2; ++c)
#pragma unroll
      for (int r = 0; r < 16; ++r) acc[a][c][r] = 0.f;
#pragma unroll
  for (int k = 0; k < 14; ++k) {
    if (k + PF < 14) K4_LOAD(k + PF);
    if (k + PF == 14) {
#pragma unroll
      for (int a = 0; a < 2; ++a)
        xr[a] = ((const u32x2*)(ws + WS_X1F))[((((size_t)(grp * 4 + mh * 2 + a) * 28 + (nn >> 4)) * 64 + ((nn >> 3) & 1) * 32 + mr) << 1) + (c4 & 1)];
#pragma unroll
      for (int q = 0; q < 4; ++q) gbp[q] = *(const f32x4*)((const float*)(ws + WS_MOD) + q * 2688 + 5 * D_ + nn);
      SB();
    }
    acc[0][0] = mfma32(wd[k][0], af[k][0], acc[0][0]);
    acc[1][0] = mfma32(wd[k][0], af[k][1], acc[1][0]);
    acc[0][1] = mfma32(wd[k][1], af[k][0], acc[0][1]);
    acc[1][1] = mfma32(wd[k][1], af[k][1], acc[1][1]);
    SB();
    if (k == 0) STAMP(3, 1);
  }
  STAMP(3, 2);
  f32x4* red = (f32x4*)smem;
#pragma unroll
  for (int a = 0; a < 2; ++a)
#pragma unroll
    for (int nt = 0; nt < 2; ++nt)
#pragma unroll
      for (int q = 0; q < 4; ++q) {
        const int cc = nt * 8 + q * 2 + h;
        red[(w * 2 + a) * 512 + cc * 32 + (m32 ^ cc)] =
            f32x4{acc[a][nt][4 * q], acc[a][nt][4 * q + 1], acc[a][nt][4 * q + 2], acc[a][nt][4 * q + 3]};
      }
  STAMP(3, 3);
  bar();
  STAMP(3, 4);
  const f32x4 gs = (gbp[0] + gbp[1]) + (gbp[2] + gbp[3]);
  const float gb[4] = {rb1(gs[0]), rb1(gs[1]), rb1(gs[2]), rb1(gs[3])};
#pragma unroll
  for (int a = 0; a < 2; ++a) {
    f32x4 pv[8];
#pragma unroll
    for (int ww = 0; ww < 8; ++ww) pv[ww] = red[(ww * 2 + a) * 512 + c4 * 32 + (mr ^ c4)];
    const f32x4 s = ((pv[0] + pv[1]) + (pv[2] + pv[3])) + ((pv[4] + pv[5]) + (pv[6] + pv[7]));
    const int mu = grp * 128 + mh * 64 + a * 32 + mr;
    const int t = (mu & ~255) | ((mu & 15) << 4) | ((mu >> 4) & 8) | ((mu >> 2) & 4) | ((mu >> 5) & 3);
    const float xv[4] = {bflo(xr[a].x), bfhi(xr[a].x), bflo(xr[a].y), bfhi(xr[a].y)};
    float o[4];
#pragma unroll
    for (int r = 0; r < 4; ++r) o[r] = __builtin_fmaf(gb[r], s[r], xv[r]);
    out[((size_t)t * D_ + nn) >> 2] = u32x2{pk2(o[0], o[1]), pk2(o[2], o[3])};
  }
  STAMPB(3, 5);
}

extern "C" void kernel_launch(void* const* d_in, const int* in_sizes, int n_in, void* d_out, int out_size, void* d_ws,
                              size_t ws_size, hipStream_t stream) {
  (void)in_sizes; (void)n_in; (void)out_size; (void)ws_size;
  const void* A_q = d_in[0];
  const void* A_k = d_in[1];
  const void* A_v = d_in[2];
  const void* A_shift = d_in[3];
  const void* A_ssmk = d_in[4];
  const void* A_ssmD = d_in[5];
  const void* W_gate = d_in[7];
  const void* W_up = d_in[8];
  const void* W_down = d_in[9];
  const void* cvec = d_in[10];
  const void* w_mod = d_in[11];
  unsigned char* ws = (unsigned char*)d_ws;

  (void)hipFuncSetAttribute((const void*)k2_conv, hipFuncAttributeMaxDynamicSharedMemorySize, K2_LDS);
  (void)hipFuncSetAttribute((const void*)k3_gemm1, hipFuncAttributeMaxDynamicSharedMemorySize, K3_LDS);
  (void)hipFuncSetAttribute((const void*)k4_gemm2, hipFuncAttributeMaxDynamicSharedMemorySize, K4_LDS);

  k1_prep<<<K1_TR + K1_MOD + K1_ST, 256, 0, stream>>>((const u32*)A_q, (const u32*)A_k, (const u32*)A_v,
                                                       (const u32x4*)A_shift, (const unsigned short*)cvec,
                                                       (const u32x4*)w_mod, ws);
  k2_conv<<<K2_CONV + K2_PREP, 256, K2_LDS, stream>>>((const u32x4*)A_ssmk, (const unsigned short*)A_ssmD,
                                        (const u32x2*)W_gate, (const u32x2*)W_up, ws);
  k3_gemm1<<<256, 512, K3_LDS, stream>>>((const u32*)W_down, ws);
  k4_gemm2<<<224, 512, K4_LDS, stream>>>(ws, (u32x2*)d_out);
}
```

```cpp
#include <hip/hip_runtime.h>
#include <hip/hip_bf16.h>

typedef unsigned int u32;
typedef u32 u32x2 __attribute__((ext_vector_type(2)));
typedef u32 u32x4 __attribute__((ext_vector_type(4)));
typedef float f32x2 __attribute__((ext_vector_type(2)));
typedef float f32x4 __attribute__((ext_vector_type(4)));
typedef float f32x16 __attribute__((ext_vector_type(16)));
typedef __bf16 bf16x2 __attribute__((ext_vector_type(2)));
typedef __bf16 bf16x8 __attribute__((ext_vector_type(8)));

#if defined(__HIP_DEVICE_COMPILE__)
#define NOPK __attribute__((target("no-packed-fp32-ops")))
#else
#define NOPK
#endif
#define DEV NOPK __device__ __forceinline__
#define SB() __builtin_amdgcn_sched_barrier(0)

#define STAMP(kid, slot)
#define STAMPB(kid, slot)

constexpr int S_ = 2048, D_ = 448, F_ = 1792;
constexpr float EPS_ = 1e-5f;

constexpr size_t WS_MOD = 0;
constexpr size_t WS_STAT = 49152;
constexpr size_t WS_FLG = 65536;
constexpr size_t WS_C12 = 81920;
constexpr size_t WS_UX = 196608;
constexpr size_t WS_X1F = WS_UX + (size_t)224 * 16384;
constexpr size_t WS_WGU = WS_X1F + (size_t)128 * 14 * 1024;
constexpr size_t WS_WD = WS_WGU + (size_t)112 * 2 * 14 * 1024;
constexpr size_t WS_ACT = WS_WD + (size_t)14 * 112 * 1024;
constexpr size_t WS_END = WS_ACT + (size_t)64 * 112 * 1024;

DEV float bflo(u32 v) { return __builtin_bit_cast(float, v << 16); }
DEV float bfhi(u32 v) { return __builtin_bit_cast(float, v & 0xffff0000u); }
DEV u32 pk2(float a, float b) {
  f32x2 v = {a, b};
  bf16x2 r = __builtin_convertvector(v, bf16x2);
  return __builtin_bit_cast(u32, r);
}
DEV f32x2 up2(u32 v) { return f32x2{bflo(v), bfhi(v)}; }
DEV f32x2 rb2(f32x2 v) { return up2(pk2(v.x, v.y)); }
DEV float rb1(float a) { return bflo(pk2(a, a)); }
DEV float bf2f(unsigned short b) { return __builtin_bit_cast(float, (u32)b << 16); }

DEV f32x4 mfma16(u32x4 a, u32x4 b, f32x4 c) {
  return __builtin_amdgcn_mfma_f32_16x16x32_bf16(__builtin_bit_cast(bf16x8, a), __builtin_bit_cast(bf16x8, b), c, 0, 0, 0);
}
DEV f32x16 mfma32(u32x4 a, u32x4 b, f32x16 c) {
  return __builtin_amdgcn_mfma_f32_32x32x16_bf16(__builtin_bit_cast(bf16x8, a), __builtin_bit_cast(bf16x8, b), c, 0, 0, 0);
}
DEV float dot2(u32 a, u32 b, float c) {
  return __builtin_amdgcn_fdot2_f32_bf16(__builtin_bit_cast(bf16x2, a), __builtin_bit_cast(bf16x2, b), c, false);
}
template <int CTRL> DEV float dppf(float v) {
  return __builtin_bit_cast(float, __builtin_amdgcn_update_dpp(0, __builtin_bit_cast(int, v), CTRL, 0xf, 0xf, true));
}
DEV float row16_sum(float v) {
  v += dppf<0xB1>(v);
  v += dppf<0x4E>(v);
  v += dppf<0x141>(v);
  v += dppf<0x140>(v);
  return v;
}
DEV float rdlane(float v, int l) {
  return __builtin_bit_cast(float, __builtin_amdgcn_readlane(__builtin_bit_cast(int, v), l));
}
DEV float wave_sum(float v) {
  v = row16_sum(v);
  return (rdlane(v, 0) + rdlane(v, 16)) + (rdlane(v, 32) + rdlane(v, 48));
}
DEV float col4_sum(float v) {
  u32 a = __builtin_bit_cast(u32, v);
  auto r = __builtin_amdgcn_permlane16_swap(a, a, false, false);
  v = __builtin_bit_cast(float, (u32)r[0]) + __builtin_bit_cast(float, (u32)r[1]);
  a = __builtin_bit_cast(u32, v);
  auto q = __builtin_amdgcn_permlane32_swap(a, a, false, false);
  return __builtin_bit_cast(float, (u32)q[0]) + __builtin_bit_cast(float, (u32)q[1]);
}
DEV void bar() {
  asm volatile("s_waitcnt lgkmcnt(0)" ::: "memory");
  __builtin_amdgcn_s_barrier();
  asm volatile("" ::: "memory");
}
typedef __attribute__((address_space(3))) u32 lu32;
DEV void lds_arrive(lu32* p) {
  asm volatile("s_waitcnt lgkmcnt(0)" ::: "memory");
  if ((threadIdx.x & 63) == 0) __hip_atomic_fetch_add(p, 1u, __ATOMIC_RELAXED, __HIP_MEMORY_SCOPE_WORKGROUP);
}
DEV void lds_wait(lu32* p, u32 want) {
#pragma nounroll
  for (unsigned spins = 0; spins < (1u << 22); ++spins) {
    const u32 v = __hip_atomic_load(p, __ATOMIC_RELAXED, __HIP_MEMORY_SCOPE_WORKGROUP);
    if (__builtin_amdgcn_readfirstlane(v) >= want) break;
    __builtin_amdgcn_s_sleep(1);
  }
  asm volatile("" ::: "memory");
}
#define CODE_PREFETCH(sym, nthreads_x16_bytes_index)                                                              \
  do {                                                                                                            \
    u32 lo_, hi_;                                                                                                 \
    asm volatile("s_getpc_b64 s[20:21]\n\ts_add_u32 s20, s20, " #sym "@rel32@lo+4\n\ts_addc_u32 s21, s21, " #sym   \
                 "@rel32@hi+12\n\ts_mov_b32 %0, s20\n\ts_mov_b32 %1, s21"                                          \
                 : "=s"(lo_), "=s"(hi_)::"s20", "s21", "scc");                                                    \
    const u32x4* cp_ = (const u32x4*)(((unsigned long long)hi_ << 32) | lo_);                                     \
    const u32x4 v_ = cp_[nthreads_x16_bytes_index];                                                               \
    asm volatile("" ::"v"(v_));                                                                                   \
  } while (0)
DEV float ln_sd(float var) { return rb1(__builtin_amdgcn_sqrtf(rb1(rb1(var) + EPS_))); }
DEV float ln_rsd(float var) { return __builtin_amdgcn_rcpf(ln_sd(var)); }
DEV void rows_bcast(float v, float (&o)[4]) {
  const u32 a = __builtin_bit_cast(u32, v);
  auto r = __builtin_amdgcn_permlane16_swap(a, a, false, false);
  auto e = __builtin_amdgcn_permlane32_swap((u32)r[0], (u32)r[0], false, false);
  auto d = __builtin_amdgcn_permlane32_swap((u32)r[1], (u32)r[1], false, false);
  o[0] = __builtin_bit_cast(float, (u32)e[0]);
  o[2] = __builtin_bit_cast(float, (u32)e[1]);
  o[1] = __builtin_bit_cast(float, (u32)d[0]);
  o[3] = __builtin_bit_cast(float, (u32)d[1]);
}

constexpr int K1_TR = 224;
constexpr int K1_MOD = 168;
constexpr int K1_ST = 128;

extern "C" NOPK __global__ void __launch_bounds__(256) k1_prep(const u32* __restrict__ xq, const u32* __restrict__ kin,
                                                    const u32* __restrict__ vin, const u32x4* __restrict__ shiftk,
                                                    const unsigned short* __restrict__ cvec,
                                                    const u32x4* __restrict__ wmod, unsigned char* __restrict__ ws) {
  __shared__ float red[32 * 65];
  asm volatile("" ::"s"(xq), "s"(kin), "s"(vin), "s"(shiftk), "s"(cvec), "s"(wmod), "s"(ws));
  const int tid = threadIdx.x;
  const int b = blockIdx.x;
  STAMPB(0, 0);
  if (b < K1_TR) {
    const int rg = b / 7, cg = b - rg * 7;
    const int p = cg * 32 + (tid & 31), j = tid >> 5;
    const int t0 = rg * 64 + j * 8;
    u32 kk[11], vv[8], xx[8];
#pragma unroll
    for (int r = 0; r < 11; ++r) {
      const int t = t0 - 3 + r;
      kk[r] = kin[(size_t)(t < 0 ? 0 : t) * 224 + p];
    }
#pragma unroll
    for (int r = 0; r < 8; ++r) vv[r] = vin[(size_t)(t0 + r) * 224 + p];
    const u32x4 sw = shiftk[p];
#pragma unroll
    for (int r = 0; r < 8; ++r) xx[r] = xq[(size_t)(t0 + r) * 224 + p];
    SB();
    if (t0 == 0) { kk[0] = 0u; kk[1] = 0u; kk[2] = 0u; }
    const f32x2 w0 = {bflo(sw.x), bflo(sw.z)}, w1 = {bfhi(sw.x), bfhi(sw.z)};
    const f32x2 w2 = {bflo(sw.y), bflo(sw.w)}, w3 = {bfhi(sw.y), bfhi(sw.w)};
    f32x2 kf[11];
#pragma unroll
    for (int r = 0; r < 11; ++r) kf[r] = up2(kk[r]);
    u32 kv[8];
#pragma unroll
    for (int r = 0; r < 8; ++r) {
      float a0 = kf[r].x * w3.x, a1 = kf[r].y * w3.y;
      a0 = __builtin_fmaf(kf[r + 1].x, w2.x, a0); a1 = __builtin_fmaf(kf[r + 1].y, w2.y, a1);
      a0 = __builtin_fmaf(kf[r + 2].x, w1.x, a0); a1 = __builtin_fmaf(kf[r + 2].y, w1.y, a1);
      a0 = __builtin_fmaf(kf[r + 3].x, w0.x, a0); a1 = __builtin_fmaf(kf[r + 3].y, w0.y, a1);
      kv[r] = pk2(a0 * bflo(vv[r]), a1 * bfhi(vv[r]));
    }
    u32x4 u0, u1, x0, x1;
#pragma unroll
    for (int i = 0; i < 4; ++i) {
      u0[i] = __builtin_amdgcn_perm(kv[6 - 2 * i], kv[7 - 2 * i], 0x05040100u);
      u1[i] = __builtin_amdgcn_perm(kv[6 - 2 * i], kv[7 - 2 * i], 0x07060302u);
      x0[i] = __builtin_amdgcn_perm(xx[2 * i + 1], xx[2 * i], 0x05040100u);
      x1[i] = __builtin_amdgcn_perm(xx[2 * i + 1], xx[2 * i], 0x07060302u);
    }
    u32x4* dst = (u32x4*)(ws + WS_UX) + ((size_t)p * 64 + rg * 2 + (j >> 2)) * 16 + (j & 3);
    dst[0] = u0;
    dst[8] = u1;
    dst[4] = x0;
    dst[12] = x1;
    STAMPB(0, 2);
    return;
  }
  if (b < K1_TR + K1_MOD) {
    const int mb = b - K1_TR;
    const int cgp = mb >> 2, rq = mb & 3;
    const int c8 = tid & 7, rg = tid >> 3;
    u32x4 w[4];
    unsigned short cs[4];
#pragma unroll
    for (int i = 0; i < 4; ++i) {
      const int rr = rg + 32 * i;
      w[i] = wmod[(size_t)(rq * 112 + (rr < 112 ? rr : 0)) * 336 + cgp * 8 + c8];
    }
#pragma unroll
    for (int i = 0; i < 4; ++i) {
      const int rr = rg + 32 * i;
      cs[i] = cvec[rq * 112 + (rr < 112 ? rr : 0)];
    }
    SB();
    float acc[8];
#pragma unroll
    for (int j = 0; j < 8; ++j) acc[j] = 0.f;
#pragma unroll
    for (int i = 0; i < 4; ++i) {
      const float c = bf2f(cs[i]);
      float sc = rb1(c * __builtin_amdgcn_rcpf(1.0f + __builtin_amdgcn_exp2f(-1.4426950408889634f * c)));
      if (rg + 32 * i >= 112) sc = 0.f;
#pragma unroll
      for (int j = 0; j < 4; ++j) {
        acc[2 * j] = __builtin_fmaf(sc, bflo(w[i][j]), acc[2 * j]);
        acc[2 * j + 1] = __builtin_fmaf(sc, bfhi(w[i][j]), acc[2 * j + 1]);
      }
    }
#pragma unroll
    for (int j = 0; j < 8; ++j) red[rg * 65 + c8 * 8 + j] = acc[j];
    bar();
    const int col = tid >> 2, part = tid & 3;
    float s = 0.f;
#pragma unroll
    for (int i = 0; i < 8; ++i) s += red[(part * 8 + i) * 65 + col];
    s += dppf<0xB1>(s);
    s += dppf<0x4E>(s);
    if (part == 0) ((float*)(ws + WS_MOD))[rq * 2688 + cgp * 64 + col] = s;
    STAMPB(0, 1);
    return;
  }
  {
    const int sb = b - K1_MOD - K1_TR;
    const int lane = tid & 63, w = tid >> 6;
    const int r0 = sb * 16 + w * 4;
    const int lc = lane < 56 ? lane : 55;
    if (sb < 16) CODE_PREFETCH(k2_conv, (sb >> 3) * 256 + tid);
    u32x4 xv[4];
#pragma unroll
    for (int r = 0; r < 4; ++r) xv[r] = ((const u32x4*)xq)[(size_t)(r0 + r) * 56 + lc];
    SB();
    float mean[4], var[4];
#pragma unroll
    for (int r = 0; r < 4; ++r) {
      float s = 0.f;
#pragma unroll
      for (int j = 0; j < 4; ++j) s += bflo(xv[r][j]) + bfhi(xv[r][j]);
      if (lane >= 56) s = 0.f;
      mean[r] = wave_sum(s) * (1.0f / D_);
    }
#pragma unroll
    for (int r = 0; r < 4; ++r) {
      float q = 0.f;
#pragma unroll
      for (int j = 0; j < 4; ++j) {
        const float a = bflo(xv[r][j]) - mean[r], bb = bfhi(xv[r][j]) - mean[r];
        q = __builtin_fmaf(a, a, q);
        q = __builtin_fmaf(bb, bb, q);
      }
      if (lane >= 56) q = 0.f;
      var[r] = wave_sum(q) * (1.0f / D_);
    }
    const float mm = lane == 0 ? mean[0] : lane == 1 ? mean[1] : lane == 2 ? mean[2] : mean[3];
    const float vv = lane == 0 ? var[0] : lane == 1 ? var[1] : lane == 2 ? var[2] : var[3];
    const float rs = ln_rsd(vv);
    if (lane < 4) ((f32x2*)(ws + WS_STAT))[r0 + lane] = f32x2{rb1(mm), rs};
    STAMPB(0, 3);
  }
}

constexpr int CV_CS = 4160;
constexpr int CV_UOFF = 4 * CV_CS;
constexpr int CV_CH = CV_UOFF + 286 * 16 + 32;
constexpr int CV_RED = 2 * CV_CH;
constexpr int K2_LDS = CV_RED + 32768;
constexpr int K2_CONV = 224, K2_PREP = 224;

extern "C" NOPK __global__ void __launch_bounds__(256, 2) k2_conv(const u32x4* __restrict__ ssmk, const unsigned short* __restrict__ ssmD,
                                                       const u32x2* __restrict__ wgate, const u32x2* __restrict__ wup,
                                                       unsigned char* __restrict__ ws) {
  extern __shared__ __attribute__((aligned(16))) unsigned char smem[];
  asm volatile("" ::"s"(ssmk), "s"(ssmD), "s"(wgate), "s"(wup), "s"(ws));
  const int b = blockIdx.x;
  const int tid = threadIdx.x;
  const float* __restrict__ mod = (const float*)(ws + WS_MOD);
  const int w = __builtin_amdgcn_readfirstlane(tid >> 6);
  const int lane = tid & 63;
  STAMPB(1, 0);
  if (b >= K2_CONV) {
    const int unit = b - K2_CONV;
    const int mat = unit / 112, fg = (unit % 112) >> 2, kq = unit & 3;
    const u32x2* __restrict__ W = mat ? wup : wgate;
    float* pm = (float*)smem + w * 64;
    f32x4* pred = (f32x4*)(smem + 1024);
    const int fq = lane & 15, ko = w * 4 + (lane >> 4);
    const int kc = ko < 14 ? ko : 13;
    u32x2 wv[8];
    float mq[4];
    {
      const int kl = w * 32 + (lane & 31);
      const int mi = (lane < 32 ? 4 * D_ : 3 * D_) + kq * 112 + (kl < 112 ? kl : 111);
#pragma unroll
      for (int q = 0; q < 4; ++q) mq[q] = mod[q * 2688 + mi];
    }
#pragma unroll
    for (int j = 0; j < 8; ++j) wv[j] = W[(size_t)(kq * 112 + kc * 8 + j) * 448 + fg * 16 + fq];
    SB();
    {
      float mv = rb1((mq[0] + mq[1]) + (mq[2] + mq[3]));
      if (lane < 32) mv = rb1(1.0f + mv);
      pm[lane] = mv;
    }
    const float* pk = pm + (lane >> 4) * 8;
    const f32x4 scA = *(const f32x4*)(pk), scB = *(const f32x4*)(pk + 4);
    const f32x4 sbA = *(const f32x4*)(pk + 32), sbB = *(const f32x4*)(pk + 36);
    const float sc[8] = {scA[0], scA[1], scA[2], scA[3], scB[0], scB[1], scB[2], scB[3]};
    const float sb[8] = {sbA[0], sbA[1], sbA[2], sbA[3], sbB[0], sbB[1], sbB[2], sbB[3]};
    float c1[4] = {0.f, 0.f, 0.f, 0.f}, c2[4] = {0.f, 0.f, 0.f, 0.f};
    u32x4 o[4];
#pragma unroll
    for (int jj = 0; jj < 4; ++jj) {
      const float a[4] = {bflo(wv[2 * jj].x), bfhi(wv[2 * jj].x), bflo(wv[2 * jj].y), bfhi(wv[2 * jj].y)};
      const float bb[4] = {bflo(wv[2 * jj + 1].x), bfhi(wv[2 * jj + 1].x), bflo(wv[2 * jj + 1].y), bfhi(wv[2 * jj + 1].y)};
#pragma unroll
      for (int f = 0; f < 4; ++f) {
        const u32 pp = pk2(a[f] * sc[2 * jj], bb[f] * sc[2 * jj + 1]);
        o[f][jj] = pp;
        c1[f] += bflo(pp) + bfhi(pp);
        c2[f] = __builtin_fmaf(a[f], sb[2 * jj], c2[f]);
        c2[f] = __builtin_fmaf(bb[f], sb[2 * jj + 1], c2[f]);
      }
    }
    {
      u32x4* tl = (u32x4*)(smem + 4096) + w * 288;
#pragma unroll
      for (int f = 0; f < 4; ++f) tl[((fq >> 2) * 4 + (lane >> 4)) * 18 + (fq & 3) * 4 + f] = o[f];
      const int ksl = lane >> 5;
      if (w * 2 + ksl < 7) {
        u32x4* dst = (u32x4*)(ws + WS_WGU) + ((size_t)(fg * 4) * 28 + kq * 7 + w * 2 + ksl) * 64 + mat * 32 + (lane & 31);
#pragma unroll
        for (int t = 0; t < 4; ++t) dst[(size_t)t * 28 * 64] = tl[(t * 4 + (lane >> 4)) * 18 + (lane & 15)];
      }
    }
    f32x4 s1, s2;
#pragma unroll
    for (int f = 0; f < 4; ++f) {
      s1[f] = col4_sum(ko < 14 ? c1[f] : 0.f);
      s2[f] = col4_sum(ko < 14 ? c2[f] : 0.f);
    }
    if (lane < 32) pred[w * 32 + lane] = lane < 16 ? s1 : s2;
    bar();
    if (unit < 24) CODE_PREFETCH(k3_gemm1, (unit >> 3) * 256 + tid);
    if (tid < 32)
      *(f32x4*)((float*)(ws + WS_C12) + (size_t)(kq * 4 + mat * 2 + (tid >> 4)) * F_ + fg * 64 + fq * 4) =
          (pred[tid] + pred[32 + tid]) + (pred[64 + tid] + pred[96 + tid]);
    STAMPB(1, 1);
    return;
  }

  __builtin_amdgcn_s_setprio(3);
  const int unit = (b & 7) * 28 + (b >> 3);
  const int d0 = 2 * unit;
  const int n = lane & 15, g = lane >> 4;
  const u32x4* __restrict__ ux = (const u32x4*)(ws + WS_UX) + (size_t)unit * 1024;
  f32x4* red = (f32x4*)(smem + CV_RED);
  const int ch = w >> 1;
  const int i = tid & 127;
  unsigned char* cb = smem + ch * CV_CH;
  const u32x4* kr = ssmk + (size_t)(d0 + ch) * 256;
  u32x4 ka[2], kb[2], uu[2];
  ka[0] = kr[i > 0 ? i - 1 : 0];
  kb[0] = kr[i];
  ka[1] = kr[i + 127];
  kb[1] = kr[i + 128];
  uu[0] = ux[(i >> 2) * 16 + ch * 8 + (i & 3)];
  uu[1] = ux[(32 + (i >> 2)) * 16 + ch * 8 + (i & 3)];
  SB();
  if (i < 34) {
    unsigned char* zp = i < 4 ? cb + i * CV_CS : cb + CV_UOFF + (256 + i - 4) * 16;
    *(u32x4*)zp = u32x4{0u, 0u, 0u, 0u};
  }
  if (i == 0) ka[0] = u32x4{0u, 0u, 0u, 0u};
#pragma unroll
  for (int it = 0; it < 2; ++it) {
    const int qp = i + 1 + 128 * it;
    const u32 Dw[8] = {ka[it].x, ka[it].y, ka[it].z, ka[it].w, kb[it].x, kb[it].y, kb[it].z, kb[it].w};
    u32 E[7];
#pragma unroll
    for (int j = 0; j < 7; ++j) E[j] = __builtin_amdgcn_alignbit(Dw[j + 1], Dw[j], 16);
    unsigned char* p = cb + qp * 16;
#pragma unroll
    for (int s2 = 0; s2 < 2; ++s2) {
      *(u32x4*)(p + (2 * s2) * CV_CS) = u32x4{E[s2], E[s2 + 1], E[s2 + 2], E[s2 + 3]};
      *(u32x4*)(p + (2 * s2 + 1) * CV_CS) = u32x4{Dw[s2 + 1], Dw[s2 + 2], Dw[s2 + 3], Dw[s2 + 4]};
    }
    if (it == 1 && i == 127) {
#pragma unroll
      for (int s2 = 0; s2 < 2; ++s2) {
        *(u32x2*)(p + 16 + (2 * s2) * CV_CS) = u32x2{E[s2 + 4], E[s2 + 5]};
        *(u32x2*)(p + 16 + (2 * s2 + 1) * CV_CS) = u32x2{Dw[s2 + 5], Dw[s2 + 6]};
      }
    }
  }
#pragma unroll
  for (int it = 0; it < 2; ++it) *(u32x4*)(cb + CV_UOFF + (255 - (i + 128 * it)) * 16) = uu[it];
  SB();
  STAMP(1, 2);
  bar();
  STAMP(1, 3);

  const int sh = w & 1;
  const unsigned char* ap = cb + (n & 3) * CV_CS + ((n >> 2) + 2 * g) * 8 + sh * 256;
  const unsigned char* bp = cb + CV_UOFF + (254 - 2 * n + g) * 16 - 3584 + sh * 256;
  f32x4 acc[8];
#pragma unroll
  for (int c = 0; c < 8; ++c) acc[c] = f32x4{0.f, 0.f, 0.f, 0.f};
  u32x2 xe[2][2];
  f32x4 st[2][2];
  float mpv;
  u32 DDr;
  u32x2 Al[2][8], Ah[2][8];
  u32x4 B[2][8];
  typedef __attribute__((address_space(3))) const unsigned char* lcp;
  typedef __attribute__((address_space(3))) const u32x2* lcp2;
  lcp ap0 = (lcp)ap, ap1 = (lcp)ap + 8;
  asm volatile("" : "+v"(ap1));
#define LDA(buf, e, s) { Al[buf][e] = *(lcp2)(ap0 + 64 * (s) + 512 * (e)); SB(); Ah[buf][e] = *(lcp2)(ap1 + 64 * (s) + 512 * (e)); }
#define LDB(buf, e, s) { B[buf][e] = *(const u32x4*)(bp + 64 * (s) + 512 * (7 - (e))); }
  LDA(0, 0, 0);
  SB();
#pragma unroll
  for (int e = 0; e < 8; ++e) { LDB(0, e, 0); SB(); }
#pragma unroll
  for (int e = 1; e < 8; ++e) { LDA(0, e, 0); SB(); }
#pragma unroll
  for (int s = 0; s < 4; ++s) {
    const int cu = s & 1, nx = cu ^ 1;
    int cnt = 0;
#pragma unroll
    for (int ii = 0; ii < 8; ++ii) {
#pragma unroll
      for (int c = ii; c < 8; ++c) {
        acc[c] = mfma16(u32x4{Al[cu][ii].x, Al[cu][ii].y, Ah[cu][ii].x, Ah[cu][ii].y}, B[cu][c - ii], acc[c]);
        if (s < 3 && cnt >= 2 && cnt < 34 && (cnt & 1) == 0) {
          const int e = (cnt - 2) >> 1;
          if (e == 0) LDA(nx, 0, s + 1)
          else if (e <= 8) LDB(nx, e - 1, s + 1)
          else LDA(nx, e - 8, s + 1)
        }
        SB();
        ++cnt;
      }
      if (s == 3 && ii > 0) { red[(w * 8 + ii - 1) * 64 + lane] = acc[ii - 1]; SB(); }
      if (s == 1 && ii < 2) {
        const int c2 = ii;
        const int t0 = 256 * (2 * w + c2) + 16 * n + 4 * g;
#pragma unroll
        for (int cc = 0; cc < 2; ++cc) xe[c2][cc] = ((const u32x2*)(ux + (t0 >> 5) * 16 + cc * 8 + 4 + ((t0 >> 3) & 3)))[g & 1];
        st[c2][0] = ((const f32x4*)(ws + WS_STAT))[t0 / 2];
        st[c2][1] = ((const f32x4*)(ws + WS_STAT))[t0 / 2 + 1];
        SB();
      }
      if (s == 1 && ii == 2) {
        const int lc = lane < 24 ? lane : 23;
        mpv = mod[(lc & 3) * 2688 + (lc >> 3) * D_ + d0 + ((lc >> 2) & 1)];
        DDr = ((const u32*)ssmD)[unit + (lane >> 6)];
        SB();
      }
    }
  }
  red[(w * 8 + 7) * 64 + lane] = acc[7];
  mpv += dppf<0xB1>(mpv);
  mpv += dppf<0x4E>(mpv);
  mpv = rb1(mpv);
  if ((lane >> 3) == 1) mpv = rb1(mpv + 1.0f);
  STAMP(1, 4);
  bar();
  STAMP(1, 5);
  {
    const f32x2 SA = {rdlane(mpv, 0), rdlane(mpv, 4)};
    const f32x2 CA = {rdlane(mpv, 8), rdlane(mpv, 12)};
    const f32x2 GA = {rdlane(mpv, 16), rdlane(mpv, 20)};
    const f32x2 DD = up2(DDr);
    u32* x1f = (u32*)(ws + WS_X1F);
    const int ks = d0 >> 4, hk = (d0 >> 3) & 1, jj = (d0 & 7) >> 1;
#pragma unroll
    for (int c2 = 0; c2 < 2; ++c2) {
      const int ct = 2 * w + c2;
      const int t0 = 256 * ct + 16 * n + 4 * g;
      const f32x4 y0 = red[(0 * 8 + ct) * 64 + lane] + red[(1 * 8 + ct) * 64 + lane];
      const f32x4 y1 = red[(2 * 8 + ct) * 64 + lane] + red[(3 * 8 + ct) * 64 + lane];
      const int uo = CV_UOFF + (255 - (t0 >> 3)) * 16 + (1 - (g & 1)) * 8;
      const u32x2 ur0 = *(const u32x2*)(smem + uo);
      const u32x2 ur1 = *(const u32x2*)(smem + CV_CH + uo);
      const float u0[4] = {bfhi(ur0.y), bflo(ur0.y), bfhi(ur0.x), bflo(ur0.x)};
      const float u1[4] = {bfhi(ur1.y), bflo(ur1.y), bfhi(ur1.x), bflo(ur1.x)};
      const float xa[4] = {bflo(xe[c2][0].x), bfhi(xe[c2][0].x), bflo(xe[c2][0].y), bfhi(xe[c2][0].y)};
      const float xb[4] = {bflo(xe[c2][1].x), bfhi(xe[c2][1].x), bflo(xe[c2][1].y), bfhi(xe[c2][1].y)};
      const float mn[4] = {st[c2][0].x, st[c2][0].z, st[c2][1].x, st[c2][1].z};
      const float rs[4] = {st[c2][0].y, st[c2][0].w, st[c2][1].y, st[c2][1].w};
      u32* dst = x1f + (((size_t)(8 * ct + 4 * (g >> 1)) * 28 + ks) * 64 + hk * 32 + (g & 1) * 16 + n) * 4 + jj;
#pragma unroll
      for (int r = 0; r < 4; ++r) {
        const float h0 = __builtin_fmaf(xa[r] - mn[r], rs[r] * CA.x, SA.x);
        const float h1 = __builtin_fmaf(xb[r] - mn[r], rs[r] * CA.y, SA.y);
        const float z0 = __builtin_fmaf(u0[r], DD.x, y0[r]);
        const float z1 = __builtin_fmaf(u1[r], DD.y, y1[r]);
        dst[(size_t)r * 28 * 256] = pk2(__builtin_fmaf(h0 * GA.x, z0, xa[r]), __builtin_fmaf(h1 * GA.y, z1, xb[r]));
      }
    }
  }
  STAMPB(1, 6);
}

constexpr int K3_XB = 28 * 4096;
constexpr int K3_ST = K3_XB + 4096;
constexpr int K3_LDS = K3_ST + 2048;

DEV u32x4 bld(__amdgpu_buffer_rsrc_t rs, int voff, int soff) { return __builtin_amdgcn_raw_buffer_load_b128(rs, voff, soff, 0); }

extern "C" NOPK __global__ void __launch_bounds__(512, 2) k3_gemm1(const u32* __restrict__ wdown, unsigned char* __restrict__ ws) {
  extern __shared__ __attribute__((aligned(16))) unsigned char smem[];
  asm volatile("" ::"s"(wdown), "s"(ws));
  const int tid = threadIdx.x;
  const int b = blockIdx.x;
  const int xcd = b & 7, idx = b >> 3;
  STAMPB(2, 0);
  if (b >= 224) {
    const int pb = b - 224;
    u32x4* dst = (u32x4*)(ws + WS_WD);
    u32 wv[4][8];
#pragma unroll
    for (int it = 0; it < 4; ++it) {
      const int li = it * 512 + tid;
      const int item = pb * 1568 + (li < 1568 ? li : 0);
      const int fo = item / 224, np = item - fo * 224;
#pragma unroll
      for (int j = 0; j < 8; ++j)
        wv[it][j] = wdown[(size_t)((fo >> 1) * 16 + (fo & 1) * 4 + (j & 3) + 8 * (j >> 2)) * 224 + np];
    }
    SB();
#pragma unroll
    for (int it = 0; it < 4; ++it) {
      const int li = it * 512 + tid;
      if (li < 1568) {
        const int item = pb * 1568 + li;
        const int fo = item / 224, np = item - fo * 224;
        u32x4 o0, o1;
#pragma unroll
        for (int j = 0; j < 4; ++j) {
          o0[j] = __builtin_amdgcn_perm(wv[it][2 * j + 1], wv[it][2 * j], 0x05040100u);
          o1[j] = __builtin_amdgcn_perm(wv[it][2 * j + 1], wv[it][2 * j], 0x07060302u);
        }
        const int nn = 2 * np;
        const int fs = fo >> 1;
        u32x4* p = dst + (((size_t)(nn >> 6) * 112 + (fs & 7) * 14 + (fs >> 3)) * 2 + ((nn >> 5) & 1)) * 64 + (fo & 1) * 32 + (nn & 31);
        p[0] = o0;
        p[1] = o1;
      }
    }
    if (pb < 8 && tid < 200) CODE_PREFETCH(k4_gemm2, tid);
    return;
  }
  const int grp = 4 * (xcd & 3) + idx / 7, Fs = 7 * (xcd >> 2) + idx % 7;
  const int lane = tid & 63;
  const int w = __builtin_amdgcn_readfirstlane(tid >> 6);
  const int ft = Fs * 8 + w;
  const __amdgpu_buffer_rsrc_t rs = __builtin_amdgcn_make_buffer_rsrc((void*)ws, 0, (int)WS_END, 0x00020000);
  const int vo = lane * 16;
  const int woff = (int)WS_WGU + ft * 28 * 1024;
  const int vw = (((lane >> 4) & 1) * 32 + (lane >> 5) * 16 + (lane & 15)) * 16;
  const int q = w & 3, par = w >> 2;
  const int xoff = (int)WS_X1F + ((grp * 4 + q) * 28 + par) * 1024;
  u32x4* xl = (u32x4*)smem;
  u32x4* xw = xl + (par * 4 + q) * 64 + lane;
  const u32x4* xp = xl + lane;
  f32x2* stl = (f32x2*)(smem + K3_ST);
  const int m32 = lane & 31, h = lane >> 5;

  u32x4 xs[14], wf[29];
#define K3_PFW 6
  constexpr int PF = K3_PFW;
#pragma unroll
  for (int i = 0; i < 2; ++i) { xs[i] = bld(rs, vo, xoff + i * 2048); SB(); }
#pragma unroll
  for (int k = 0; k < 2; ++k) { wf[k] = bld(rs, vw, woff + k * 1024); SB(); }
  __builtin_amdgcn_s_barrier();
#pragma unroll
  for (int i = 2; i < 4; ++i) {
    wf[i] = bld(rs, vw, woff + i * 1024);
    xs[i] = bld(rs, vo, xoff + i * 2048);
    SB();
  }

  const u32 one = (((lane >> 4) ^ lane) & 1) ? 0u : 0x3f803f80u;
  const u32x4 pat = {one, one, one, one};
  f32x4 sm = {0.f, 0.f, 0.f, 0.f};
  float sq[4] = {0.f, 0.f, 0.f, 0.f};
#define XSTAT(kk)                                                           \
  {                                                                         \
    sm = mfma16(pat, xs[kk], sm);                                           \
    _Pragma("unroll") for (int j = 0; j < 4; ++j) {                         \
      const float lo = bflo(xs[kk][j]), hi = bfhi(xs[kk][j]);               \
      sq[j] = __builtin_fmaf(lo, lo, sq[j]);                                \
      sq[j ^ 1] = __builtin_fmaf(hi, hi, sq[j ^ 1]);                        \
    }                                                                       \
    asm volatile("" : "+v"(sq[0]), "+v"(sq[1]), "+v"(sq[2]), "+v"(sq[3])); \
  }
#pragma unroll
  for (int i = 0; i < 2; ++i) xw[i * 512] = xs[i];
  STAMP(2, 1);
  bar();
  STAMP(2, 2);
#pragma unroll
  for (int k = 4; k < PF; ++k) { wf[k] = bld(rs, vw, woff + k * 1024); SB(); }

  f32x16 ac[4];
#pragma unroll
  for (int m = 0; m < 4; ++m)
#pragma unroll
    for (int r = 0; r < 16; ++r) ac[m][r] = 0.f;
  u32x4 xf[2][4];
#pragma unroll
  for (int m = 0; m < 4; ++m) xf[0][m] = xp[m * 64];
  SB();
  float cq[2][4];
#pragma unroll
  for (int k = 0; k < 24; ++k) {
    const int cu = k & 1, nx = cu ^ 1, c = k >> 2;
    if (k + PF < 28) wf[k + PF] = bld(rs, vw, woff + (k + PF) * 1024);
    if ((k & 3) < 2 && 2 * c + 4 + (k & 3) < 14) xs[2 * c + 4 + (k & 3)] = bld(rs, vo, xoff + (2 * c + 4 + (k & 3)) * 2048);
    if (k == 20) {
      const float* c12 = (const float*)(ws + WS_C12) + ((m32 >> 4) * 2) * F_ + ft * 16 + (m32 & 15);
#pragma unroll
      for (int e = 0; e < 4; ++e) { cq[0][e] = c12[e * 4 * F_]; cq[1][e] = c12[e * 4 * F_ + F_]; }
    }
    SB();
    if ((k & 3) == 3) {
      if (k == 23) {
        XSTAT(12);
        XSTAT(13);
        float s2 = (sq[0] + sq[1]) + (sq[2] + sq[3]);
        const u32 a = __builtin_bit_cast(u32, s2);
        auto r = __builtin_amdgcn_permlane32_swap(a, a, false, false);
        s2 = __builtin_bit_cast(float, (u32)r[0]) + __builtin_bit_cast(float, (u32)r[1]);
        const float s1 = (lane & 16) ? sm[1] : sm[0];
        if (lane < 32) stl[par * 128 + q * 32 + lane] = f32x2{s1, s2};
      }
      bar();
      SB();
    }
#pragma unroll
    for (int m = 0; m < 4; ++m) {
      if (k < 23) xf[nx][m] = xp[((k + 1) * 4 + m) * 64];
      else xf[nx][m] = xp[((24 + m) * 4 + 0) * 64];
      ac[m] = mfma32(wf[k], xf[cu][m], ac[m]);
      if (m == 1 && (k & 3) < 2) XSTAT(2 * c + (k & 3));
      if (m == 3 && (k & 3) == 2) { xw[(2 * c + 2) * 512] = xs[2 * c + 2]; xw[(2 * c + 3) * 512] = xs[2 * c + 3]; }
      SB();
    }
  }
  STAMP(2, 3);
  {
    constexpr float GK0 = -2.0f * 0.7978845608028654f * 1.4426950408889634f;
    const int acts = (int)WS_ACT + ((grp * 4) * 112 + ft) * 1024;
    float sd[4];
    u32x4 bx[4];
    f32x2 pa[4], pb[4];
#pragma unroll
    for (int m = 0; m < 4; ++m) { pa[m] = stl[m * 32 + m32]; pb[m] = stl[128 + m * 32 + m32]; }
    SB();
    float et[8], eq[8], K0 = 0.f, K1 = 0.f, S2 = 0.f;
#pragma unroll
    for (int m = 0; m < 5; ++m) {
#pragma unroll
      for (int j = 0; j < 5; ++j) {
        if (m < 4) {
          if (j < 4) {
            const int sidx = m * 4 + j, nxt = sidx + 4;
            const u32x4 xc = xf[0][j];
            ac[m] = mfma32(wf[24 + j], xc, ac[m]);
            if (nxt < 16) xf[0][j] = xp[((24 + j) * 4 + m + 1) * 64];
          } else ac[m] = mfma32(wf[28], bx[m], ac[m]);
        }
        if (m == 0) {
          if (j < 2) {
#pragma unroll
            for (int t = 2 * j; t < 2 * j + 2; ++t) {
              const float mean = (pa[t].x + pb[t].x) * (1.0f / D_);
              const float var = __builtin_fmaxf(__builtin_fmaf(-mean, mean, (pa[t].y + pb[t].y) * (1.0f / D_)), 0.f);
              sd[t] = ln_sd(var);
              bx[t] = u32x4{h ? 0u : pk2(sd[t], sd[t]), h ? 0u : pk2(-mean, -mean), 0u, 0u};
            }
          }
          if (j == 2) {
            const float c1 = (cq[0][0] + cq[0][1]) + (cq[0][2] + cq[0][3]), c2 = (cq[1][0] + cq[1][1]) + (cq[1][2] + cq[1][3]);
            const float c1h = rb1(c1), c2h = rb1(c2);
            wf[28] = u32x4{pk2(c2h, c2 - c2h), pk2(c1h, c1 - c1h), 0u, 0u};
          }
        } else {
          const int t = m - 1;
          if (j == 0) {
            const float rsd = __builtin_amdgcn_rcpf(sd[t]);
            K0 = GK0 * rsd;
            K1 = K0 * 0.044715f * rsd * rsd;
            S2 = sd[t] * sd[t];
          }
#pragma unroll
          for (int r = 0; r < 8; ++r) if (j == 0) et[r] = ac[t][r] * ac[t][r];
#pragma unroll
          for (int r = 0; r < 8; ++r) if (j == 1) et[r] = __builtin_fmaf(et[r], K1, K0);
#pragma unroll
          for (int r = 0; r < 8; ++r) if (j == 1) et[r] = ac[t][r] * et[r];
#pragma unroll
          for (int r = 0; r < 8; ++r) if (j == 2) { et[r] = __builtin_amdgcn_exp2f(et[r]); eq[r] = ac[t][r] * ac[t][r + 8]; }
#pragma unroll
          for (int r = 0; r < 8; ++r) if (j == 3) et[r] = __builtin_fmaf(et[r], S2, S2);
#pragma unroll
          for (int r = 0; r < 8; ++r) if (j == 3) et[r] = __builtin_amdgcn_rcpf(et[r]);
#pragma unroll
          for (int r = 0; r < 8; ++r) if (j == 4) eq[r] = eq[r] * et[r];
          if (j == 4)
            __builtin_amdgcn_raw_buffer_store_b128(u32x4{pk2(eq[0], eq[1]), pk2(eq[2], eq[3]), pk2(eq[4], eq[5]), pk2(eq[6], eq[7])},
                                                   rs, vo, acts + t * 112 * 1024, 0);
        }
        SB();
      }
      if (m == 3) STAMP(2, 4);
    }
  }
  STAMPB(2, 5);
}

constexpr int K4_LDS = 131072;

extern "C" NOPK __global__ void __launch_bounds__(512, 2) k4_gemm2(unsigned char* __restrict__ ws, u32x2* __restrict__ out) {
  extern __shared__ __attribute__((aligned(16))) unsigned char smem[];
  const int tid = threadIdx.x;
  const int b = blockIdx.x;
  const int xcd = b & 7, idx = b >> 3;
  STAMPB(3, 0);
  const int grp = 4 * (xcd & 3) + idx / 7, mh = xcd >> 2, nt64 = idx % 7;
  const int lane = tid & 63;
  const int w = __builtin_amdgcn_readfirstlane(tid >> 6);
  const __amdgpu_buffer_rsrc_t rs = __builtin_amdgcn_make_buffer_rsrc((void*)ws, 0, (int)WS_END, 0x00020000);
  const int vo = lane * 16;
  const int doff = (int)WS_WD + (nt64 * 112 + 14 * w) * 2048;
  const int aoff = (int)WS_ACT + ((grp * 4 + mh * 2) * 112 + w) * 1024;
  const int m32 = lane & 31, h = lane >> 5;
  constexpr int PF = 3;
  u32x4 wd[14][2], af[14][2];
#define K4_LOAD(k)                                                   \
  {                                                                  \
    af[k][0] = bld(rs, vo, aoff + (k) * 8192);                       \
    wd[k][0] = bld(rs, vo, doff + (k) * 2048);                       \
    af[k][1] = bld(rs, vo, aoff + 112 * 1024 + (k) * 8192);          \
    wd[k][1] = bld(rs, vo, doff + (k) * 2048 + 1024);                \
    SB();                                                            \
  }
#pragma unroll
  for (int k = 0; k < PF; ++k) K4_LOAD(k);
  const int mr = tid >> 4, c4 = tid & 15;
  const int nn = nt64 * 64 + 4 * c4;
  u32x2 xr[2];
  f32x4 gbp[4];
  f32x16 acc[2][2];
#pragma unroll
  for (int a = 0; a < 2; ++a)
#pragma unroll
    for (int c = 0; c < 2; ++c)
#pragma unroll
      for (int r = 0; r < 16; ++r) acc[a][c][r] = 0.f;
#pragma unroll
  for (int k = 0; k < 14; ++k) {
    if (k + PF < 14) K4_LOAD(k + PF);
    if (k + PF == 14) {
#pragma unroll
      for (int a = 0; a < 2; ++a)
        xr[a] = ((const u32x2*)(ws + WS_X1F))[((((size_t)(grp * 4 + mh * 2 + a) * 28 + (nn >> 4)) * 64 + ((nn >> 3) & 1) * 32 + mr) << 1) + (c4 & 1)];
#pragma unroll
      for (int q = 0; q < 4; ++q) gbp[q] = *(const f32x4*)((const float*)(ws + WS_MOD) + q * 2688 + 5 * D_ + nn);
      SB();
    }
    acc[0][0] = mfma32(wd[k][0], af[k][0], acc[0][0]);
    acc[1][0] = mfma32(wd[k][0], af[k][1], acc[1][0]);
    acc[0][1] = mfma32(wd[k][1], af[k][0], acc[0][1]);
    acc[1][1] = mfma32(wd[k][1], af[k][1], acc[1][1]);
    SB();
    if (k == 0) STAMP(3, 1);
  }
  STAMP(3, 2);
  f32x4* red = (f32x4*)smem;
#pragma unroll
  for (int a = 0; a < 2; ++a)
#pragma unroll
    for (int nt = 0; nt < 2; ++nt)
#pragma unroll
      for (int q = 0; q < 4; ++q) {
        const int cc = nt * 8 + q * 2 + h;
        red[(w * 2 + a) * 512 + cc * 32 + (m32 ^ cc)] =
            f32x4{acc[a][nt][4 * q], acc[a][nt][4 * q + 1], acc[a][nt][4 * q + 2], acc[a][nt][4 * q + 3]};
      }
  STAMP(3, 3);
  bar();
  STAMP(3, 4);
  const f32x4 gs = (gbp[0] + gbp[1]) + (gbp[2] + gbp[3]);
  const float gb[4] = {rb1(gs[0]), rb1(gs[1]), rb1(gs[2]), rb1(gs[3])};
#pragma unroll
  for (int a = 0; a < 2; ++a) {
    f32x4 pv[8];
#pragma unroll
    for (int ww = 0; ww < 8; ++ww) pv[ww] = red[(ww * 2 + a) * 512 + c4 * 32 + (mr ^ c4)];
    const f32x4 s = ((pv[0] + pv[1]) + (pv[2] + pv[3])) + ((pv[4] + pv[5]) + (pv[6] + pv[7]));
    const int mu = grp * 128 + mh * 64 + a * 32 + mr;
    const int t = (mu & ~255) | ((mu & 15) << 4) | ((mu >> 4) & 8) | ((mu >> 2) & 4) | ((mu >> 5) & 3);
    const float xv[4] = {bflo(xr[a].x), bfhi(xr[a].x), bflo(xr[a].y), bfhi(xr[a].y)};
    float o[4];
#pragma unroll
    for (int r = 0; r < 4; ++r) o[r] = __builtin_fmaf(gb[r], s[r], xv[r]);
    out[((size_t)t * D_ + nn) >> 2] = u32x2{pk2(o[0], o[1]), pk2(o[2], o[3])};
  }
  STAMPB(3, 5);
}

extern "C" void kernel_launch(void* const* d_in, const int* in_sizes, int n_in, void* d_out, int out_size, void* d_ws,
                              size_t ws_size, hipStream_t stream) {
  (void)in_sizes; (void)n_in; (void)out_size; (void)ws_size;
  const void* A_q = d_in[0];
  const void* A_k = d_in[1];
  const void* A_v = d_in[2];
  const void* A_shift = d_in[3];
  const void* A_ssmk = d_in[4];
  const void* A_ssmD = d_in[5];
  const void* W_gate = d_in[7];
  const void* W_up = d_in[8];
  const void* W_down = d_in[9];
  const void* cvec = d_in[10];
  const void* w_mod = d_in[11];
  unsigned char* ws = (unsigned char*)d_ws;

  (void)hipFuncSetAttribute((const void*)k2_conv, hipFuncAttributeMaxDynamicSharedMemorySize, K2_LDS);
  (void)hipFuncSetAttribute((const void*)k3_gemm1, hipFuncAttributeMaxDynamicSharedMemorySize, K3_LDS);
  (void)hipFuncSetAttribute((const void*)k4_gemm2, hipFuncAttributeMaxDynamicSharedMemorySize, K4_LDS);

  k1_prep<<<K1_TR + K1_MOD + K1_ST, 256, 0, stream>>>((const u32*)A_q, (const u32*)A_k, (const u32*)A_v,
                                                       (const u32x4*)A_shift, (const unsigned short*)cvec,
                                                       (const u32x4*)w_mod, ws);
  k2_conv<<<K2_CONV + K2_PREP, 256, K2_LDS, stream>>>((const u32x4*)A_ssmk, (const unsigned short*)A_ssmD,
                                        (const u32x2*)W_gate, (const u32x2*)W_up, ws);
  k3_gemm1<<<256, 512, K3_LDS, stream>>>((const u32*)W_down, ws);
  k4_gemm2<<<224, 512, K4_LDS, stream>>>(ws, (u32x2*)d_out);
}
```

```cpp
#include <hip/hip_runtime.h>
#include <hip/hip_bf16.h>

typedef unsigned int u32;
typedef u32 u32x2 __attribute__((ext_vector_type(2)));
typedef u32 u32x4 __attribute__((ext_vector_type(4)));
typedef float f32x2 __attribute__((ext_vector_type(2)));
typedef float f32x4 __attribute__((ext_vector_type(4)));
typedef float f32x16 __attribute__((ext_vector_type(16)));
typedef __bf16 bf16x2 __attribute__((ext_vector_type(2)));
typedef __bf16 bf16x8 __attribute__((ext_vector_type(8)));

#if defined(__HIP_DEVICE_COMPILE__)
#define NOPK __attribute__((target("no-packed-fp32-ops")))
#else
#define NOPK
#endif
#define DEV NOPK __device__ __forceinline__
#define SB() __builtin_amdgcn_sched_barrier(0)

constexpr int D_ = 448, F_ = 1792;
constexpr float EPS_ = 1e-5f;

constexpr size_t WS_MOD = 0;
constexpr size_t WS_STAT = 49152;
constexpr size_t WS_FLG = 65536;
constexpr size_t WS_GRN = 73728;
constexpr size_t WS_C12 = 90112;
constexpr size_t WS_UX = 204800;
constexpr size_t WS_X1F = WS_UX + (size_t)224 * 16384;
constexpr size_t WS_WGU = WS_X1F + (size_t)128 * 14 * 1024;
constexpr size_t WS_WD = WS_WGU + (size_t)112 * 2 * 14 * 1024;
constexpr size_t WS_ACT = WS_WD + (size_t)14 * 112 * 1024;
constexpr size_t WS_END = WS_ACT + (size_t)64 * 112 * 1024;

DEV float bflo(u32 v) { return __builtin_bit_cast(float, v << 16); }
DEV float bfhi(u32 v) { return __builtin_bit_cast(float, v & 0xffff0000u); }
DEV u32 pk2(float a, float b) {
  f32x2 v = {a, b};
  bf16x2 r = __builtin_convertvector(v, bf16x2);
  return __builtin_bit_cast(u32, r);
}
DEV f32x2 up2(u32 v) { return f32x2{bflo(v), bfhi(v)}; }
DEV f32x2 rb2(f32x2 v) { return up2(pk2(v.x, v.y)); }
DEV float rb1(float a) { return bflo(pk2(a, a)); }
DEV float bf2f(unsigned short b) { return __builtin_bit_cast(float, (u32)b << 16); }

DEV f32x4 mfma16(u32x4 a, u32x4 b, f32x4 c) {
  return __builtin_amdgcn_mfma_f32_16x16x32_bf16(__builtin_bit_cast(bf16x8, a), __builtin_bit_cast(bf16x8, b), c, 0, 0, 0);
}
DEV f32x16 mfma32(u32x4 a, u32x4 b, f32x16 c) {
  return __builtin_amdgcn_mfma_f32_32x32x16_bf16(__builtin_bit_cast(bf16x8, a), __builtin_bit_cast(bf16x8, b), c, 0, 0, 0);
}
template <int CTRL> DEV float dppf(float v) {
  return __builtin_bit_cast(float, __builtin_amdgcn_update_dpp(0, __builtin_bit_cast(int, v), CTRL, 0xf, 0xf, true));
}
DEV float row16_sum(float v) {
  v += dppf<0xB1>(v);
  v += dppf<0x4E>(v);
  v += dppf<0x141>(v);
  v += dppf<0x140>(v);
  return v;
}
DEV float rdlane(float v, int l) {
  return __builtin_bit_cast(float, __builtin_amdgcn_readlane(__builtin_bit_cast(int, v), l));
}
DEV float wave_sum(float v) {
  v = row16_sum(v);
  return (rdlane(v, 0) + rdlane(v, 16)) + (rdlane(v, 32) + rdlane(v, 48));
}
DEV float col4_sum(float v) {
  u32 a = __builtin_bit_cast(u32, v);
  auto r = __builtin_amdgcn_permlane16_swap(a, a, false, false);
  v = __builtin_bit_cast(float, (u32)r[0]) + __builtin_bit_cast(float, (u32)r[1]);
  a = __builtin_bit_cast(u32, v);
  auto q = __builtin_amdgcn_permlane32_swap(a, a, false, false);
  return __builtin_bit_cast(float, (u32)q[0]) + __builtin_bit_cast(float, (u32)q[1]);
}
DEV void bar() {
  asm volatile("s_waitcnt lgkmcnt(0)" ::: "memory");
  __builtin_amdgcn_s_barrier();
  asm volatile("" ::: "memory");
}
typedef __attribute__((address_space(1))) u32x4 gu32x4;
#define CODE_PTR(sym)                                                                                             \
  ({                                                                                                              \
    u32 lo_, hi_;                                                                                                 \
    asm volatile("s_getpc_b64 s[20:21]\n\ts_add_u32 s20, s20, " #sym "@rel32@lo+4\n\ts_addc_u32 s21, s21, " #sym   \
                 "@rel32@hi+12\n\ts_mov_b32 %0, s20\n\ts_mov_b32 %1, s21"                                          \
                 : "=s"(lo_), "=s"(hi_)::"s20", "s21", "scc");                                                    \
    (const gu32x4*)(((unsigned long long)hi_ << 32) | lo_);                                                       \
  })
DEV void put16_wt(unsigned char* ws, size_t byte_off, u32x4 v) {
  const __amdgpu_buffer_rsrc_t r = __builtin_amdgcn_make_buffer_rsrc((void*)ws, 0, (int)WS_END, 0x00020000);
  __builtin_amdgcn_raw_buffer_store_b128(v, r, (int)byte_off, 0, 16);
}
DEV int img_off(int k, int ch) { return 2048 * (k >> 3) + 512 * (ch >> 2) + 64 * (k & 7) + 16 * ((ch & 3) ^ ((k >> 2) & 3)); }
constexpr int IMG_B = 448 * 256;
DEV float ln_sd(float var) { return rb1(__builtin_amdgcn_sqrtf(rb1(rb1(var) + EPS_))); }
DEV float ln_rsd(float var) { return __builtin_amdgcn_rcpf(ln_sd(var)); }

constexpr int K2_CODE_B = 10240;
constexpr int K1_TR = 224;
constexpr int K1_MOD = 168;
constexpr int K1_ST = 128;

extern "C" __global__ void __launch_bounds__(256) k1_prep(const u32x4* __restrict__ ssmk, const u32* __restrict__ xq, const u32* __restrict__ kin,
                                                    const u32* __restrict__ vin, const u32x4* __restrict__ shiftk,
                                                    const unsigned short* __restrict__ cvec,
                                                    const u32x4* __restrict__ wmod, unsigned char* __restrict__ ws) {
  __shared__ __attribute__((aligned(16))) float red[32 * 33 * 4];
  asm volatile("" ::"s"(ssmk), "s"(xq), "s"(kin), "s"(vin), "s"(shiftk), "s"(cvec), "s"(wmod), "s"(ws));
  const int tid = threadIdx.x;
  const int b = blockIdx.x;
  if (b < K1_TR) {
    const int rg = (b & 7) * 4 + (b >> 3) / 7, cg = (b >> 3) % 7;
    const int p = cg * 32 + (tid & 31), j = tid >> 5;
    const int t0 = rg * 64 + j * 8;
    u32 kk[11], vv[8], xx[8];
#pragma unroll
    for (int r = 0; r < 11; ++r) {
      const int t = t0 - 3 + r;
      kk[r] = kin[(size_t)(t < 0 ? 0 : t) * 224 + p];
    }
#pragma unroll
    for (int r = 0; r < 8; ++r) vv[r] = vin[(size_t)(t0 + r) * 224 + p];
    const u32x4 sw = shiftk[p];
#pragma unroll
    for (int r = 0; r < 8; ++r) xx[r] = xq[(size_t)(t0 + r) * 224 + p];
    SB();
    const u32x4* kp = ssmk + (size_t)((b & 7) * 28 + (b >> 3)) * 512 + tid;
    const u32x4 pfa = kp[0], pfc = kp[256];
    SB();
    if (t0 == 0) { kk[0] = 0u; kk[1] = 0u; kk[2] = 0u; }
    const f32x2 w0 = {bflo(sw.x), bflo(sw.z)}, w1 = {bfhi(sw.x), bfhi(sw.z)};
    const f32x2 w2 = {bflo(sw.y), bflo(sw.w)}, w3 = {bfhi(sw.y), bfhi(sw.w)};
    f32x2 kf[11];
#pragma unroll
    for (int r = 0; r < 11; ++r) kf[r] = up2(kk[r]);
    u32 kv[8];
#pragma unroll
    for (int r = 0; r < 8; ++r) {
      f32x2 a = rb2(kf[r] * w3);
      a = rb2(a + rb2(kf[r + 1] * w2));
      a = rb2(a + rb2(kf[r + 2] * w1));
      a = rb2(a + rb2(kf[r + 3] * w0));
      const f32x2 pr = a * up2(vv[r]);
      kv[r] = pk2(pr.x, pr.y);
    }
    u32x4 u0, u1, x0, x1;
#pragma unroll
    for (int i = 0; i < 4; ++i) {
      u0[i] = __builtin_amdgcn_perm(kv[6 - 2 * i], kv[7 - 2 * i], 0x05040100u);
      u1[i] = __builtin_amdgcn_perm(kv[6 - 2 * i], kv[7 - 2 * i], 0x07060302u);
      x0[i] = __builtin_amdgcn_perm(xx[2 * i + 1], xx[2 * i], 0x05040100u);
      x1[i] = __builtin_amdgcn_perm(xx[2 * i + 1], xx[2 * i], 0x07060302u);
    }
    {
      u32x4* tl = (u32x4*)red;
      u32x4* tw = tl + (tid & 31) * 33 + (j >> 2) * 16 + (j & 3);
      tw[0] = u0;
      tw[8] = u1;
      tw[4] = x0;
      tw[12] = x1;
      bar();
#pragma unroll
      for (int i = 0; i < 4; ++i) {
        const int pl = i * 8 + (tid >> 5), sl = tid & 31;
        put16_wt(ws, WS_UX + (((size_t)(cg * 32 + pl) * 64 + rg * 2) * 16 + sl) * 16, tl[pl * 33 + sl]);
      }
    }
    asm volatile("" ::"v"(pfa), "v"(pfc));
    return;
  }
  if (b < K1_TR + K1_MOD) {
    const int mb = b - K1_TR;
    const int cgp = mb >> 2, rq = mb & 3;
    const int c8 = tid & 7, rg = tid >> 3;
    u32x4 w[4];
    unsigned short cs[4];
#pragma unroll
    for (int i = 0; i < 4; ++i) {
      const int rr = rg + 32 * i;
      w[i] = wmod[(size_t)(rq * 112 + (rr < 112 ? rr : 0)) * 336 + cgp * 8 + c8];
    }
#pragma unroll
    for (int i = 0; i < 4; ++i) {
      const int rr = rg + 32 * i;
      cs[i] = cvec[rq * 112 + (rr < 112 ? rr : 0)];
    }
    SB();
    float acc[8];
#pragma unroll
    for (int j = 0; j < 8; ++j) acc[j] = 0.f;
#pragma unroll
    for (int i = 0; i < 4; ++i) {
      const float c = bf2f(cs[i]);
      float sc = rb1(c * __builtin_amdgcn_rcpf(1.0f + __builtin_amdgcn_exp2f(-1.4426950408889634f * c)));
      if (rg + 32 * i >= 112) sc = 0.f;
#pragma unroll
      for (int j = 0; j < 4; ++j) {
        acc[2 * j] = __builtin_fmaf(sc, bflo(w[i][j]), acc[2 * j]);
        acc[2 * j + 1] = __builtin_fmaf(sc, bfhi(w[i][j]), acc[2 * j + 1]);
      }
    }
#pragma unroll
    for (int j = 0; j < 8; ++j) red[rg * 65 + c8 * 8 + j] = acc[j];
    bar();
    const int col = tid >> 2, part = tid & 3;
    float s = 0.f;
#pragma unroll
    for (int i = 0; i < 8; ++i) s += red[(part * 8 + i) * 65 + col];
    s += dppf<0xB1>(s);
    s += dppf<0x4E>(s);
    if (part == 0) ((float*)(ws + WS_MOD))[rq * 2688 + cgp * 64 + col] = s;
    return;
  }
  {
    const int sb = b - K1_MOD - K1_TR;
    const int lane = tid & 63, w = tid >> 6;
    const int r0 = ((sb & 7) * 16 + (sb >> 3)) * 16 + w * 4;
    const int lc = lane < 56 ? lane : 55;
    if (tid < 48) ((u32*)(ws + WS_FLG))[sb * 48 + tid] = 0u;
    u32x4 xv[4];
#pragma unroll
    for (int r = 0; r < 4; ++r) xv[r] = ((const u32x4*)xq)[(size_t)(r0 + r) * 56 + lc];
    SB();
    const int ci = (sb >> 3) * 256 + tid;
    const u32x4 nextpf = CODE_PTR(k2_conv)[ci < K2_CODE_B / 16 ? ci : 0];
    SB();
    float mean[4], var[4];
#pragma unroll
    for (int r = 0; r < 4; ++r) {
      float s = 0.f;
#pragma unroll
      for (int j = 0; j < 4; ++j) s += bflo(xv[r][j]) + bfhi(xv[r][j]);
      if (lane >= 56) s = 0.f;
      mean[r] = wave_sum(s) * (1.0f / D_);
    }
#pragma unroll
    for (int r = 0; r < 4; ++r) {
      float q = 0.f;
#pragma unroll
      for (int j = 0; j < 4; ++j) {
        const float a = bflo(xv[r][j]) - mean[r], bb = bfhi(xv[r][j]) - mean[r];
        q = __builtin_fmaf(a, a, q);
        q = __builtin_fmaf(bb, bb, q);
      }
      if (lane >= 56) q = 0.f;
      var[r] = wave_sum(q) * (1.0f / D_);
    }
    const float mm = lane == 0 ? mean[0] : lane == 1 ? mean[1] : lane == 2 ? mean[2] : mean[3];
    const float vv = lane == 0 ? var[0] : lane == 1 ? var[1] : lane == 2 ? var[2] : var[3];
    const float rs = ln_rsd(vv);
    if (lane < 4) ((f32x2*)(ws + WS_STAT))[r0 + lane] = f32x2{rb1(mm), rs};
    asm volatile("" ::"v"(nextpf));
  }
}

constexpr int K34_CODE_B = 12288;
constexpr int CV_CS = 4160;
constexpr int CV_UOFF = 4 * CV_CS;
constexpr int CV_CH = CV_UOFF + 286 * 16 + 32;
constexpr int CV_RED = 2 * CV_CH;
constexpr int K2_LDS = CV_RED + 32768;
constexpr int K2_CONV = 224, K2_WD = 32, K2_PREP = 224;

extern "C" NOPK __global__ void __launch_bounds__(512, 4) k2_conv(const u32x4* __restrict__ ssmk, const unsigned short* __restrict__ ssmD,
                                                       const u32x2* __restrict__ wgate, const u32x2* __restrict__ wup,
                                                       const u32* __restrict__ wdown, unsigned char* __restrict__ ws) {
  extern __shared__ __attribute__((aligned(16))) unsigned char smem[];
  asm volatile("" ::"s"(ssmk), "s"(ssmD), "s"(wgate), "s"(wup), "s"(wdown), "s"(ws));
  const int b = blockIdx.x;
  const int tid = threadIdx.x;
  const float* __restrict__ mod = (const float*)(ws + WS_MOD);
  const int w = __builtin_amdgcn_readfirstlane(tid >> 6);
  const int lane = tid & 63;
  if (b >= K2_CONV && b < K2_CONV + K2_WD) {
    const int u0 = ((b - K2_CONV) * 8 + w) * 4;
    if (u0 >= 784) return;
    const int hh = lane >> 5, pi = lane & 31;
    u32x4* tl = (u32x4*)smem + w * 128;
    u32 wv[4][8];
#pragma unroll
    for (int it = 0; it < 4; ++it) {
      const int fs = (u0 + it) / 7, nt64 = (u0 + it) - fs * 7;
#pragma unroll
      for (int j = 0; j < 8; ++j) wv[it][j] = wdown[(size_t)(fs * 16 + hh * 4 + (j & 3) + 8 * (j >> 2)) * 224 + nt64 * 32 + pi];
    }
    SB();
#pragma unroll
    for (int it = 0; it < 4; ++it) {
      const int fs = (u0 + it) / 7, nt64 = (u0 + it) - fs * 7;
      u32x4 o0, o1;
#pragma unroll
      for (int j = 0; j < 4; ++j) {
        o0[j] = __builtin_amdgcn_perm(wv[it][2 * j + 1], wv[it][2 * j], 0x05040100u);
        o1[j] = __builtin_amdgcn_perm(wv[it][2 * j + 1], wv[it][2 * j], 0x07060302u);
      }
      const int slot = (pi >> 4) * 64 + hh * 32 + (pi & 15) * 2;
      tl[slot] = o0;
      tl[slot + 1] = o1;
      const u32x4 r0 = tl[lane], r1 = tl[64 + lane];
      const size_t po = WS_WD + ((size_t)(nt64 * 112 + (fs & 7) * 14 + (fs >> 3)) * 128 + lane) * 16;
      put16_wt(ws, po, r0);
      put16_wt(ws, po + 1024, r1);
      SB();
    }
    return;
  }
  if (b >= K2_CONV) {
    const int unit = b - K2_CONV - K2_WD, pw = w;
    if (w >= 4) {
      const int ci = (unit >> 3) * 256 + tid - 256;
      if (ci < K34_CODE_B / 16) {
        const u32x4 nextpf = CODE_PTR(k34_ffn)[ci];
        asm volatile("" ::"v"(nextpf));
      }
      return;
    }
    __builtin_amdgcn_s_setprio(1);
    const int mat = unit / 112, fg = (unit % 112) >> 2, kq = unit & 3;
    const u32x2* __restrict__ W = mat ? wup : wgate;
    float* pm = (float*)smem + w * 64;
    f32x4* pred = (f32x4*)(smem + 2048);
    const int fq = lane & 15, ko = pw * 4 + (lane >> 4);
    const int kc = ko < 14 ? ko : 13;
    u32x2 wv[8];
    float mq[4];
    {
      const int kl = pw * 32 + (lane & 31);
      const int mi = (lane < 32 ? 4 * D_ : 3 * D_) + kq * 112 + (kl < 112 ? kl : 111);
#pragma unroll
      for (int q = 0; q < 4; ++q) mq[q] = mod[q * 2688 + mi];
    }
#pragma unroll
    for (int j = 0; j < 8; ++j) wv[j] = W[(size_t)(kq * 112 + kc * 8 + j) * 448 + fg * 16 + fq];
    SB();
    {
      float mv = rb1((mq[0] + mq[1]) + (mq[2] + mq[3]));
      if (lane < 32) mv = rb1(1.0f + mv);
      pm[lane] = mv;
    }
    const float* pk = pm + (lane >> 4) * 8;
    const f32x4 scA = *(const f32x4*)(pk), scB = *(const f32x4*)(pk + 4);
    const f32x4 sbA = *(const f32x4*)(pk + 32), sbB = *(const f32x4*)(pk + 36);
    const float sc[8] = {scA[0], scA[1], scA[2], scA[3], scB[0], scB[1], scB[2], scB[3]};
    const float sb[8] = {sbA[0], sbA[1], sbA[2], sbA[3], sbB[0], sbB[1], sbB[2], sbB[3]};
    float c1[4] = {0.f, 0.f, 0.f, 0.f}, c2[4] = {0.f, 0.f, 0.f, 0.f};
    u32x4 o[4];
#pragma unroll
    for (int jj = 0; jj < 4; ++jj) {
      const float a[4] = {bflo(wv[2 * jj].x), bfhi(wv[2 * jj].x), bflo(wv[2 * jj].y), bfhi(wv[2 * jj].y)};
      const float bb[4] = {bflo(wv[2 * jj + 1].x), bfhi(wv[2 * jj + 1].x), bflo(wv[2 * jj + 1].y), bfhi(wv[2 * jj + 1].y)};
#pragma unroll
      for (int f = 0; f < 4; ++f) {
        const u32 pp = pk2(a[f] * sc[2 * jj], bb[f] * sc[2 * jj + 1]);
        o[f][jj] = pp;
        c1[f] += bflo(pp) + bfhi(pp);
        c2[f] = __builtin_fmaf(a[f], sb[2 * jj], c2[f]);
        c2[f] = __builtin_fmaf(bb[f], sb[2 * jj + 1], c2[f]);
      }
    }
    {
      u32x4* tl = (u32x4*)(smem + 8192) + w * 288;
#pragma unroll
      for (int f = 0; f < 4; ++f) tl[((fq >> 2) * 4 + (lane >> 4)) * 18 + (fq & 3) * 4 + f] = o[f];
      const int ksl = lane >> 5;
      if (pw * 2 + ksl < 7) {
        const size_t po = WS_WGU + (((size_t)(fg * 4) * 28 + kq * 7 + pw * 2 + ksl) * 64 + mat * 32 + (lane & 31)) * 16;
#pragma unroll
        for (int t = 0; t < 4; ++t) put16_wt(ws, po + (size_t)t * 28 * 1024, tl[(t * 4 + (lane >> 4)) * 18 + (lane & 15)]);
      }
    }
    f32x4 s1, s2;
#pragma unroll
    for (int f = 0; f < 4; ++f) {
      s1[f] = col4_sum(ko < 14 ? c1[f] : 0.f);
      s2[f] = col4_sum(ko < 14 ? c2[f] : 0.f);
    }
    if (lane < 32) pred[pw * 32 + lane] = lane < 16 ? s1 : s2;
    bar();
    if (pw == 0 && lane < 32)
      *(f32x4*)((float*)(ws + WS_C12) + (size_t)(kq * 4 + mat * 2 + (lane >> 4)) * F_ + fg * 64 + fq * 4) =
          (pred[lane] + pred[32 + lane]) + (pred[64 + lane] + pred[96 + lane]);
    return;
  }

  const int unit = (b & 7) * 28 + (b >> 3);
  const int d0 = 2 * unit;
  const int n = lane & 15, g = lane >> 4;
  const u32x4* __restrict__ ux = (const u32x4*)(ws + WS_UX) + (size_t)unit * 1024;
  f32x4* red = (f32x4*)(smem + CV_RED);
  if (w >= 4) {
    const int ew = w - 4;
    __builtin_amdgcn_s_setprio(2);
    bar();
    u32x2 xe[2][2];
    f32x4 st[2][2];
#pragma unroll
    for (int c2 = 0; c2 < 2; ++c2) {
      const int t0 = 256 * (2 * ew + c2) + 16 * n + 4 * g;
#pragma unroll
      for (int cc = 0; cc < 2; ++cc) xe[c2][cc] = ((const u32x2*)(ux + (t0 >> 5) * 16 + cc * 8 + 4 + ((t0 >> 3) & 3)))[g & 1];
      st[c2][0] = ((const f32x4*)(ws + WS_STAT))[t0 / 2];
      st[c2][1] = ((const f32x4*)(ws + WS_STAT))[t0 / 2 + 1];
    }
    float mpv;
    {
      const int lc = lane < 24 ? lane : 23;
      mpv = mod[(lc & 3) * 2688 + (lc >> 3) * D_ + d0 + ((lc >> 2) & 1)];
    }
    const u32 DDr = ((const u32*)ssmD)[unit + (lane >> 6)];
    SB();
    mpv += dppf<0xB1>(mpv);
    mpv += dppf<0x4E>(mpv);
    mpv = rb1(mpv);
    if ((lane >> 3) == 1) mpv = rb1(mpv + 1.0f);
    const f32x2 SA = {rdlane(mpv, 0), rdlane(mpv, 4)};
    const f32x2 CA = {rdlane(mpv, 8), rdlane(mpv, 12)};
    const f32x2 GA = {rdlane(mpv, 16), rdlane(mpv, 20)};
    const f32x2 DD = up2(DDr);
    u32 hp[2][4], tp[2][4];
#pragma unroll
    for (int c2 = 0; c2 < 2; ++c2) {
      const int t0 = 256 * (2 * ew + c2) + 16 * n + 4 * g;
      const int uo = CV_UOFF + (255 - (t0 >> 3)) * 16 + (1 - (g & 1)) * 8;
      const u32x2 ur0 = *(const u32x2*)(smem + uo);
      const u32x2 ur1 = *(const u32x2*)(smem + CV_CH + uo);
      const float u0[4] = {bfhi(ur0.y), bflo(ur0.y), bfhi(ur0.x), bflo(ur0.x)};
      const float u1[4] = {bfhi(ur1.y), bflo(ur1.y), bfhi(ur1.x), bflo(ur1.x)};
      const float xa[4] = {bflo(xe[c2][0].x), bfhi(xe[c2][0].x), bflo(xe[c2][0].y), bfhi(xe[c2][0].y)};
      const float xb[4] = {bflo(xe[c2][1].x), bfhi(xe[c2][1].x), bflo(xe[c2][1].y), bfhi(xe[c2][1].y)};
      const float mn[4] = {st[c2][0].x, st[c2][0].z, st[c2][1].x, st[c2][1].z};
      const float rs[4] = {st[c2][0].y, st[c2][0].w, st[c2][1].y, st[c2][1].w};
#pragma unroll
      for (int r = 0; r < 4; ++r) {
        const f32x2 XN = rb2(rb2(f32x2{xa[r] - mn[r], xb[r] - mn[r]}) * rs[r]);
        const f32x2 H = rb2(XN * CA) + SA;
        hp[c2][r] = pk2(H.x, H.y);
        tp[c2][r] = pk2(u0[r] * DD.x, u1[r] * DD.y);
      }
    }
    asm volatile("" : "+v"(hp[0][0]), "+v"(hp[0][1]), "+v"(hp[0][2]), "+v"(hp[0][3]), "+v"(hp[1][0]), "+v"(hp[1][1]), "+v"(hp[1][2]), "+v"(hp[1][3]));
    asm volatile("" : "+v"(tp[0][0]), "+v"(tp[0][1]), "+v"(tp[0][2]), "+v"(tp[0][3]), "+v"(tp[1][0]), "+v"(tp[1][1]), "+v"(tp[1][2]), "+v"(tp[1][3]));
    __builtin_amdgcn_s_setprio(3);
    bar();
#pragma unroll
    for (int c2 = 0; c2 < 2; ++c2) {
      const int ct = 2 * ew + c2;
      const f32x4 y0 = red[(0 * 8 + ct) * 64 + lane] + red[(1 * 8 + ct) * 64 + lane];
      const f32x4 y1 = red[(2 * 8 + ct) * 64 + lane] + red[(3 * 8 + ct) * 64 + lane];
      const float xa[4] = {bflo(xe[c2][0].x), bfhi(xe[c2][0].x), bflo(xe[c2][0].y), bfhi(xe[c2][0].y)};
      const float xb[4] = {bflo(xe[c2][1].x), bfhi(xe[c2][1].x), bflo(xe[c2][1].y), bfhi(xe[c2][1].y)};
      float r0[4], r1[4];
#pragma unroll
      for (int r = 0; r < 4; ++r) {
        const f32x2 Z = rb2(rb2(f32x2{y0[r], y1[r]}) + up2(tp[c2][r]));
        const f32x2 G = rb2(GA * rb2(up2(hp[c2][r]) * Z));
        r0[r] = xa[r] + G.x;
        r1[r] = xb[r] + G.y;
      }
      auto sx = __builtin_amdgcn_permlane16_swap(pk2(r0[0], r0[1]), pk2(r1[0], r1[1]), false, false);
      auto sy = __builtin_amdgcn_permlane16_swap(pk2(r0[2], r0[3]), pk2(r1[2], r1[3]), false, false);
      const int t8 = 256 * ct + 16 * n + 8 * (g >> 1);
      put16_wt(ws, WS_X1F + (size_t)(t8 >> 7) * IMG_B + img_off(d0 + (g & 1), (t8 & 127) >> 3),
                  u32x4{(u32)sx[0], (u32)sy[0], (u32)sx[1], (u32)sy[1]});
    }
    return;
  }
  __builtin_amdgcn_s_setprio(3);
  const int ch = w >> 1;
  const int i = tid & 127;
  unsigned char* cb = smem + ch * CV_CH;
  const u32x4* kr = ssmk + (size_t)(d0 + ch) * 256;
  u32x4 ka[2], kb[2], uu[2];
  ka[0] = kr[i > 0 ? i - 1 : 0];
  kb[0] = kr[i];
  ka[1] = kr[i + 127];
  kb[1] = kr[i + 128];
  uu[0] = ux[(i >> 2) * 16 + ch * 8 + (i & 3)];
  uu[1] = ux[(32 + (i >> 2)) * 16 + ch * 8 + (i & 3)];
  SB();
  if (i < 34) {
    unsigned char* zp = i < 4 ? cb + i * CV_CS : cb + CV_UOFF + (256 + i - 4) * 16;
    *(u32x4*)zp = u32x4{0u, 0u, 0u, 0u};
  }
  if (i == 0) ka[0] = u32x4{0u, 0u, 0u, 0u};
#pragma unroll
  for (int it = 0; it < 2; ++it) {
    const int qp = i + 1 + 128 * it;
    const u32 Dw[8] = {ka[it].x, ka[it].y, ka[it].z, ka[it].w, kb[it].x, kb[it].y, kb[it].z, kb[it].w};
    u32 E[7];
#pragma unroll
    for (int j = 0; j < 7; ++j) E[j] = __builtin_amdgcn_alignbit(Dw[j + 1], Dw[j], 16);
    unsigned char* p = cb + qp * 16;
#pragma unroll
    for (int s2 = 0; s2 < 2; ++s2) {
      *(u32x4*)(p + (2 * s2) * CV_CS) = u32x4{E[s2], E[s2 + 1], E[s2 + 2], E[s2 + 3]};
      *(u32x4*)(p + (2 * s2 + 1) * CV_CS) = u32x4{Dw[s2 + 1], Dw[s2 + 2], Dw[s2 + 3], Dw[s2 + 4]};
    }
    if (it == 1 && i == 127) {
#pragma unroll
      for (int s2 = 0; s2 < 2; ++s2) {
        *(u32x2*)(p + 16 + (2 * s2) * CV_CS) = u32x2{E[s2 + 4], E[s2 + 5]};
        *(u32x2*)(p + 16 + (2 * s2 + 1) * CV_CS) = u32x2{Dw[s2 + 5], Dw[s2 + 6]};
      }
    }
  }
#pragma unroll
  for (int it = 0; it < 2; ++it) *(u32x4*)(cb + CV_UOFF + (255 - (i + 128 * it)) * 16) = uu[it];
  SB();
  bar();

  const int sh = w & 1;
  const unsigned char* ap = cb + (n & 3) * CV_CS + ((n >> 2) + 2 * g) * 8 + sh * 256;
  const unsigned char* bp = cb + CV_UOFF + (254 - 2 * n + g) * 16 - 3584 + sh * 256;
  f32x4 acc[8];
#pragma unroll
  for (int c = 0; c < 8; ++c) acc[c] = f32x4{0.f, 0.f, 0.f, 0.f};
  u32x2 Al[32], Ah[32];
  u32x4 B[4][8];
  typedef __attribute__((address_space(3))) const unsigned char* lcp;
  typedef __attribute__((address_space(3))) const u32x2* lcp2;
  lcp ap0 = (lcp)ap, ap1 = (lcp)ap + 8;
  asm volatile("" : "+v"(ap1));
#define LDA(R) { Al[R] = *(lcp2)(ap0 + 64 * ((R) >> 3) + 512 * ((R) & 7)); SB(); Ah[R] = *(lcp2)(ap1 + 64 * ((R) >> 3) + 512 * ((R) & 7)); SB(); }
#define LDB(s, e) { B[s][e] = *(const u32x4*)(bp + 64 * (s) + 512 * (7 - (e))); SB(); }
  LDA(0);
#pragma unroll
  for (int e = 7; e >= 0; --e) LDB(0, e);
#pragma unroll
  for (int R = 1; R < 4; ++R) LDA(R);
#pragma unroll
  for (int R = 0; R < 32; ++R) {
    const int s = R >> 3, ii = R & 7;
#pragma unroll
    for (int c = 7; c >= ii; --c) {
      acc[c] = mfma16(u32x4{Al[R].x, Al[R].y, Ah[R].x, Ah[R].y}, B[s][c - ii], acc[c]);
      SB();
      if (c == 7) {
        if (s < 3) LDB(s + 1, 7 - ii);
        if (R + 4 < 32) LDA(R + 4);
      }
    }
    if (s == 3 && ii > 0) { red[(w * 8 + ii - 1) * 64 + lane] = acc[ii - 1]; SB(); }
  }
  red[(w * 8 + 7) * 64 + lane] = acc[7];
  bar();
}

constexpr int K3_ST = 131072;
constexpr int AUX_SC1 = 16;
typedef __attribute__((address_space(1))) u32 gu32;
typedef __attribute__((address_space(1))) unsigned long long gu64;
constexpr int K3_LDS = K3_ST + 8192;

DEV u32x4 bld(__amdgpu_buffer_rsrc_t rs, int voff, int soff) { return __builtin_amdgcn_raw_buffer_load_b128(rs, voff, soff, 0); }
DEV u32x4 bld_sc1(__amdgpu_buffer_rsrc_t rs, int voff, int soff) { return __builtin_amdgcn_raw_buffer_load_b128(rs, voff, soff, AUX_SC1); }
DEV void flag_wait(const unsigned char* ws, int idx) {
#pragma nounroll
  for (unsigned spins = 0; spins < (1u << 18); ++spins) {
    const u32 v = __hip_atomic_load((gu32*)(ws + WS_FLG) + idx, __ATOMIC_RELAXED, __HIP_MEMORY_SCOPE_AGENT);
    if (__builtin_amdgcn_ballot_w64(v == 0u) == 0ull) break;
    __builtin_amdgcn_s_sleep(4);
  }
  __builtin_amdgcn_fence(__ATOMIC_ACQUIRE, "wavefront");
}

extern "C" NOPK __global__ void __launch_bounds__(512, 2) k34_ffn(unsigned char* __restrict__ ws, u32x2* __restrict__ out) {
  extern __shared__ __attribute__((aligned(16))) unsigned char smem[];
  asm volatile("" ::"s"(out), "s"(ws));
  const int tid = threadIdx.x;
  const int bx_ = blockIdx.x;
  if (bx_ < 32) {
    const unsigned char* img = ws + WS_X1F + (size_t)(bx_ >> 1) * IMG_B;
    const int lc = (bx_ & 1) * 8 + (tid & 7), kk = tid >> 3;
    u32x4 xv[7];
#pragma unroll
    for (int i = 0; i < 7; ++i) xv[i] = *(const u32x4*)(img + img_off(kk + 64 * i, lc));
    SB();
    float s1[8], s2[8];
#pragma unroll
    for (int e = 0; e < 8; ++e) { s1[e] = 0.f; s2[e] = 0.f; }
#pragma unroll
    for (int i = 0; i < 7; ++i)
#pragma unroll
      for (int j = 0; j < 4; ++j) {
        const float lo = bflo(xv[i][j]), hi = bfhi(xv[i][j]);
        s1[2 * j] += lo; s1[2 * j + 1] += hi;
        s2[2 * j] = __builtin_fmaf(lo, lo, s2[2 * j]); s2[2 * j + 1] = __builtin_fmaf(hi, hi, s2[2 * j + 1]);
      }
    f32x2* part = (f32x2*)smem;
    f32x2* part2 = part + 64 * 65;
#pragma unroll
    for (int e = 0; e < 8; ++e) part[kk * 65 + (tid & 7) * 8 + e] = f32x2{s1[e], s2[e]};
    bar();
    {
      const int row = tid & 63, pp = tid >> 6;
      float a = 0.f, c = 0.f;
#pragma unroll
      for (int e = 0; e < 8; ++e) { const f32x2 v = part[(pp * 8 + e) * 65 + row]; a += v.x; c += v.y; }
      part2[pp * 64 + row] = f32x2{a, c};
    }
    bar();
    if (tid < 64) {
      float a = 0.f, c = 0.f;
#pragma unroll
      for (int e = 0; e < 8; ++e) { const f32x2 v = part2[e * 64 + tid]; a += v.x; c += v.y; }
      const float mean = a * (1.0f / D_);
      const float var = __builtin_fmaxf(__builtin_fmaf(-mean, mean, c * (1.0f / D_)), 0.f);
      const unsigned long long gr = (1ull << 32) | pk2(ln_sd(var), -mean);
      __hip_atomic_store((gu64*)(ws + WS_GRN) + bx_ * 64 + tid, gr, __ATOMIC_RELAXED, __HIP_MEMORY_SCOPE_AGENT);
    }
    return;
  }
  const int b = bx_ - 32;
  const int xcd = b & 7, idx = b >> 3;
  const int grp = 4 * (xcd & 3) + idx / 7, mh = xcd >> 2, nt64 = idx % 7, Fs = 7 * mh + nt64;
  const int lane = tid & 63;
  const int w = __builtin_amdgcn_readfirstlane(tid >> 6);
  const int ft = Fs * 8 + w;
  const __amdgpu_buffer_rsrc_t rs = __builtin_amdgcn_make_buffer_rsrc((void*)ws, 0, (int)WS_END, 0x00020000);
  const int vo = lane * 16;
  const int woff = (int)WS_WGU + ft * 28 * 1024;
  const int vw = (((lane >> 4) & 1) * 32 + (lane >> 5) * 16 + (lane & 15)) * 16;
  u32x4* xl = (u32x4*)smem;
  typedef __attribute__((address_space(3))) unsigned char* lbp;
  typedef short s16x4 __attribute__((ext_vector_type(4)));
  typedef __attribute__((address_space(3))) s16x4* ltr;
  const int trq = (lane & 15) >> 2, trp = lane & 3, trb = (lane >> 4) & 1;
  const lbp tb0 = (lbp)smem + img_off(8 * (lane >> 5) + trq, 2 * trb + (trp >> 1)) + 8 * (trp & 1);
  const lbp tb1 = (lbp)smem + img_off(8 * (lane >> 5) + 4 + trq, 2 * trb + (trp >> 1)) + 8 * (trp & 1);
#define XFRAG(ks, m)                                                                                            \
  ({                                                                                                            \
    const u32x2 lo_ = __builtin_bit_cast(u32x2, __builtin_amdgcn_ds_read_tr16_b64_v4i16((ltr)(tb0 + (ks) * 4096 + (m) * 512))); \
    const u32x2 hi_ = __builtin_bit_cast(u32x2, __builtin_amdgcn_ds_read_tr16_b64_v4i16((ltr)(tb1 + (ks) * 4096 + (m) * 512))); \
    u32x4{lo_.x, lo_.y, hi_.x, hi_.y};                                                                          \
  })
  const int m32 = lane & 31, h = lane >> 5;
  u32x4 wf[29], wd[28];
  const int doff = (int)WS_WD + (nt64 * 112 + 14 * w) * 2048;
  const int aoff = (int)WS_ACT + ((grp * 4 + mh * 2) * 112 + w) * 1024;
  const int acts = (int)WS_ACT + ((grp * 4) * 112 + ft) * 1024;
  constexpr float GK0 = -2.0f * 0.7978845608028654f * 1.4426950408889634f;
  f32x16 ac[4];
#pragma unroll
  for (int m = 0; m < 4; ++m)
#pragma unroll
    for (int r = 0; r < 16; ++r) ac[m][r] = 0.f;
  float cq[2][4];
  unsigned long long gr[4];
  gu64* grp_ = (gu64*)(ws + WS_GRN) + grp * 128 + m32;
  u32x4 xf[2][4];
#define LOAD_CQ()                                                                                              \
  {                                                                                                            \
    const float* c12 = (const float*)(ws + WS_C12) + ((m32 >> 4) * 2) * F_ + ft * 16 + (m32 & 15);             \
    _Pragma("unroll") for (int e = 0; e < 4; ++e) { cq[0][e] = c12[e * 4 * F_]; cq[1][e] = c12[e * 4 * F_ + F_]; } \
    _Pragma("unroll") for (int m = 0; m < 4; ++m) gr[m] = __hip_atomic_load(grp_ + m * 32, __ATOMIC_RELAXED, __HIP_MEMORY_SCOPE_AGENT); \
  }
#define BIAS_STEP()                                                                                            \
  {                                                                                                            \
    const float c1 = (cq[0][0] + cq[0][1]) + (cq[0][2] + cq[0][3]), c2 = (cq[1][0] + cq[1][1]) + (cq[1][2] + cq[1][3]); \
    const float c1h = rb1(c1), c2h = rb1(c2);                                                                  \
    wf[28] = u32x4{h ? 0u : pk2(c2h, c2 - c2h), h ? 0u : pk2(c1h, c1 - c1h), 0u, 0u};                           \
    if (__builtin_amdgcn_ballot_w64(((gr[0] & gr[1] & gr[2] & gr[3]) >> 32) == 0ull) != 0ull) {                \
      _Pragma("nounroll") for (unsigned sp = 0; sp < (1u << 18); ++sp) {                                       \
        __builtin_amdgcn_s_sleep(4);                                                                           \
        _Pragma("unroll") for (int m = 0; m < 4; ++m) gr[m] = __hip_atomic_load(grp_ + m * 32, __ATOMIC_RELAXED, __HIP_MEMORY_SCOPE_AGENT); \
        if (__builtin_amdgcn_ballot_w64(((gr[0] & gr[1] & gr[2] & gr[3]) >> 32) == 0ull) == 0ull) break;       \
      }                                                                                                        \
    }                                                                                                          \
    SB();                                                                                                      \
    _Pragma("unroll") for (int m = 0; m < 4; ++m) {                                                            \
      const u32 v = (u32)gr[m];                                                                                \
      ac[m] = mfma32(wf[28], u32x4{__builtin_amdgcn_perm(v, v, 0x01000100u), __builtin_amdgcn_perm(v, v, 0x03020302u), 0u, 0u}, ac[m]); \
      SB();                                                                                                    \
    }                                                                                                          \
  }
  {
    constexpr int PF = 6;
    const int q = w & 3, par = w >> 2;
    const int xoff = (int)WS_X1F + grp * IMG_B + (par * 4 + q) * 1024;
    u32x4* xw = xl + (par * 4 + q) * 64 + lane;
    u32x4 xs[14];
#pragma unroll
    for (int i = 0; i < 2; ++i) { xs[i] = bld(rs, vo, xoff + i * 8192); SB(); }
#pragma unroll
    for (int k = 0; k < 2; ++k) { wf[k] = bld(rs, vw, woff + k * 1024); SB(); }
    __builtin_amdgcn_s_barrier();
#pragma unroll
    for (int i = 2; i < 4; ++i) {
      wf[i] = bld(rs, vw, woff + i * 1024);
      xs[i] = bld(rs, vo, xoff + i * 8192);
      SB();
    }
#pragma unroll
    for (int i = 0; i < 2; ++i) xw[i * 512] = xs[i];
    bar();
#pragma unroll
    for (int k = 4; k < PF; ++k) { wf[k] = bld(rs, vw, woff + k * 1024); SB(); }
#pragma unroll
    for (int m = 0; m < 4; ++m) xf[0][m] = XFRAG(0, m);
    SB();
#pragma unroll
    for (int k = 0; k < 24; ++k) {
      const int cu = k & 1, nx = cu ^ 1;
      if (k + PF < 28) wf[k + PF] = bld(rs, vw, woff + (k + PF) * 1024);
      if (k < 10) xs[4 + k] = bld(rs, vo, xoff + (4 + k) * 8192);
      if (k >= 22) { wd[2 * (k - 22)] = bld(rs, vo, doff + 2 * (k - 22) * 1024); wd[2 * (k - 22) + 1] = bld(rs, vo, doff + (2 * (k - 22) + 1) * 1024); }
      if (k == 13) LOAD_CQ();
      SB();
      if (k == 3 || k == 7 || k == 15) { bar(); SB(); }
#pragma unroll
      for (int m = 0; m < 4; ++m) {
        if (k < 23) xf[nx][m] = XFRAG(k + 1, m);
        else xf[nx][m] = XFRAG(24 + m, 0);
        ac[m] = mfma32(wf[k], xf[cu][m], ac[m]);
        if (m == 3) {
          if (k == 2) { xw[2 * 512] = xs[2]; xw[3 * 512] = xs[3]; }
          if (k == 5 || k == 6) { xw[(2 * k - 6) * 512] = xs[2 * k - 6]; xw[(2 * k - 5) * 512] = xs[2 * k - 5]; }
          if (k >= 9 && k < 15) xw[(k - 1) * 512] = xs[k - 1];
        }
        SB();
      }
      if (k == 21) BIAS_STEP();
    }
    float et[8], eq[8], K0 = 0.f, K1 = 0.f, S2 = 0.f;
#pragma unroll
    for (int m = 0; m < 5; ++m) {
#pragma unroll
      for (int j = 0; j < 4; ++j) {
        if (m < 4) {
          const u32x4 xc = xf[0][j];
          ac[m] = mfma32(wf[24 + j], xc, ac[m]);
          if (m < 3) xf[0][j] = XFRAG(24 + j, m + 1);
          wd[4 + m * 4 + j] = bld(rs, vo, doff + (4 + m * 4 + j) * 1024);
        }
        if (m >= 1) {
          const int t = m - 1;
          if (j == 0) {
            const float sdv = bflo((u32)gr[t]);
            const float rsd = __builtin_amdgcn_rcpf(sdv);
            K0 = GK0 * rsd;
            K1 = K0 * 0.044715f * rsd * rsd;
            S2 = sdv * sdv;
          }
#pragma unroll
          for (int r = 0; r < 8; ++r) if (j == 0) et[r] = ac[t][r] * ac[t][r];
#pragma unroll
          for (int r = 0; r < 8; ++r) if (j == 0) et[r] = __builtin_fmaf(et[r], K1, K0);
#pragma unroll
          for (int r = 0; r < 8; ++r) if (j == 1) et[r] = ac[t][r] * et[r];
#pragma unroll
          for (int r = 0; r < 8; ++r) if (j == 1) et[r] = __builtin_amdgcn_exp2f(et[r]);
#pragma unroll
          for (int r = 0; r < 8; ++r) if (j == 2) { eq[r] = ac[t][r] * ac[t][r + 8]; et[r] = __builtin_fmaf(et[r], S2, S2); }
#pragma unroll
          for (int r = 0; r < 8; ++r) if (j == 2) et[r] = __builtin_amdgcn_rcpf(et[r]);
#pragma unroll
          for (int r = 0; r < 8; ++r) if (j == 3) eq[r] = eq[r] * et[r];
          if (j == 3)
            __builtin_amdgcn_raw_buffer_store_b128(u32x4{pk2(eq[0], eq[1]), pk2(eq[2], eq[3]), pk2(eq[4], eq[5]), pk2(eq[6], eq[7])},
                                                   rs, vo + acts + t * 112 * 1024, 0, AUX_SC1);
        }
        SB();
      }
    }
  }
  asm volatile("s_waitcnt vmcnt(0)" ::: "memory");
  if (lane == 0) __hip_atomic_store((gu32*)(ws + WS_FLG) + (grp * 8 + w) * 16 + Fs, 1u, __ATOMIC_RELAXED, __HIP_MEMORY_SCOPE_AGENT);
  SB();
#pragma unroll
  for (int qq = 20; qq < 28; ++qq) { wd[qq] = bld(rs, vo, doff + qq * 1024); SB(); }
  {
    const int mr_ = tid >> 4, k0 = nt64 * 64 + 4 * (tid & 15);
    u32x4 rv;
#pragma unroll
    for (int a = 0; a < 2; ++a) {
      const int m = mh * 64 + a * 32 + mr_;
      const unsigned short* e = (const unsigned short*)(smem + img_off(k0, m >> 3) + (m & 7) * 2);
      rv[2 * a] = (u32)e[0] | ((u32)e[32] << 16);
      rv[2 * a + 1] = (u32)e[64] | ((u32)e[96] << 16);
    }
    *(u32x4*)(smem + K3_ST + tid * 16) = rv;
  }
  bar();

  flag_wait(ws, (grp * 8 + w) * 16 + (lane < 14 ? lane : 13));
  constexpr int AF = 5;
  u32x4 af[14][2];
#pragma unroll
  for (int k = 0; k < AF; ++k) {
    af[k][0] = bld_sc1(rs, vo, aoff + k * 8192);
    af[k][1] = bld_sc1(rs, vo, aoff + 112 * 1024 + k * 8192);
    SB();
  }
  const int mr = tid >> 4, c4 = tid & 15;
  const int nn = nt64 * 64 + 4 * c4;
  f32x4 gbp[4];
  f32x16 acc[2][2];
#pragma unroll
  for (int a = 0; a < 2; ++a)
#pragma unroll
    for (int c = 0; c < 2; ++c)
#pragma unroll
      for (int r = 0; r < 16; ++r) acc[a][c][r] = 0.f;
#pragma unroll
  for (int k = 0; k < 14; ++k) {
    acc[0][0] = mfma32(wd[2 * k], af[k][0], acc[0][0]);
    acc[0][1] = mfma32(wd[2 * k + 1], af[k][0], acc[0][1]);
    acc[1][0] = mfma32(wd[2 * k], af[k][1], acc[1][0]);
    acc[1][1] = mfma32(wd[2 * k + 1], af[k][1], acc[1][1]);
    SB();
#pragma unroll
    for (int e = 0; e < 2; ++e) {
      const int kk = AF + 2 * k + e;
      if (kk < 14) {
        af[kk][0] = bld_sc1(rs, vo, aoff + kk * 8192);
        af[kk][1] = bld_sc1(rs, vo, aoff + 112 * 1024 + kk * 8192);
      }
    }
    if (k == 6) {
#pragma unroll
      for (int qq = 0; qq < 4; ++qq) gbp[qq] = *(const f32x4*)((const float*)(ws + WS_MOD) + qq * 2688 + 5 * D_ + nn);
    }
    SB();
  }
  f32x4* red = (f32x4*)smem;
#pragma unroll
  for (int a = 0; a < 2; ++a)
#pragma unroll
    for (int nt = 0; nt < 2; ++nt)
#pragma unroll
      for (int qq = 0; qq < 4; ++qq) {
        const int cc = nt * 8 + qq * 2 + h;
        red[(w * 2 + a) * 512 + cc * 32 + (m32 ^ cc)] =
            f32x4{acc[a][nt][4 * qq], acc[a][nt][4 * qq + 1], acc[a][nt][4 * qq + 2], acc[a][nt][4 * qq + 3]};
      }
  bar();
  const u32x4 xr = *(const u32x4*)(smem + K3_ST + tid * 16);
  const f32x4 gs = (gbp[0] + gbp[1]) + (gbp[2] + gbp[3]);
  const float gb[4] = {rb1(gs[0]), rb1(gs[1]), rb1(gs[2]), rb1(gs[3])};
#pragma unroll
  for (int a = 0; a < 2; ++a) {
    f32x4 pv[8];
#pragma unroll
    for (int ww = 0; ww < 8; ++ww) pv[ww] = red[(ww * 2 + a) * 512 + c4 * 32 + (mr ^ c4)];
    const f32x4 sv = ((pv[0] + pv[1]) + (pv[2] + pv[3])) + ((pv[4] + pv[5]) + (pv[6] + pv[7]));
    const int t = grp * 128 + mh * 64 + a * 32 + mr;
    const float xv[4] = {bflo(xr[2 * a]), bfhi(xr[2 * a]), bflo(xr[2 * a + 1]), bfhi(xr[2 * a + 1])};
    float o[4];
#pragma unroll
    for (int r = 0; r < 4; r += 2) {
      const f32x2 dn = rb2(f32x2{sv[r], sv[r + 1]});
      const f32x2 gd = rb2(f32x2{gb[r] * dn.x, gb[r + 1] * dn.y});
      o[r] = xv[r] + gd.x;
      o[r + 1] = xv[r + 1] + gd.y;
    }
    out[((size_t)t * D_ + nn) >> 2] = u32x2{pk2(o[0], o[1]), pk2(o[2], o[3])};
  }
}

extern "C" void kernel_launch(void* const* d_in, const int* in_sizes, int n_in, void* d_out, int out_size, void* d_ws,
                              size_t ws_size, hipStream_t stream) {
  (void)in_sizes; (void)n_in; (void)out_size; (void)ws_size;
  const void* A_q = d_in[0];
  const void* A_k = d_in[1];
  const void* A_v = d_in[2];
  const void* A_shift = d_in[3];
  const void* A_ssmk = d_in[4];
  const void* A_ssmD = d_in[5];
  const void* W_gate = d_in[7];
  const void* W_up = d_in[8];
  const void* W_down = d_in[9];
  const void* cvec = d_in[10];
  const void* w_mod = d_in[11];
  unsigned char* ws = (unsigned char*)d_ws;

  (void)hipFuncSetAttribute((const void*)k2_conv, hipFuncAttributeMaxDynamicSharedMemorySize, K2_LDS);
  (void)hipFuncSetAttribute((const void*)k34_ffn, hipFuncAttributeMaxDynamicSharedMemorySize, K3_LDS);

  k1_prep<<<K1_TR + K1_MOD + K1_ST, 256, 0, stream>>>((const u32x4*)A_ssmk, (const u32*)A_q, (const u32*)A_k, (const u32*)A_v,
                                                       (const u32x4*)A_shift, (const unsigned short*)cvec,
                                                       (const u32x4*)w_mod, ws);
  k2_conv<<<K2_CONV + K2_WD + K2_PREP, 512, K2_LDS, stream>>>((const u32x4*)A_ssmk, (const unsigned short*)A_ssmD,
                                        (const u32x2*)W_gate, (const u32x2*)W_up, (const u32*)W_down, ws);
  k34_ffn<<<256, 512, K3_LDS, stream>>>(ws, (u32x2*)d_out);
}
```
